```python
import torch

def _softmax(x, axis=-1):
    return torch.softmax(x, dim=axis)
def _gelu(x):
    return torch.nn.functional.gelu(x, approximate="tanh")
def _silu(x):
    return torch.nn.functional.silu(x)
def _sigmoid(x):
    return torch.sigmoid(x)
def _layernorm(x, axis=-1, eps=1e-5):
    m = x.mean(dim=axis, keepdim=True)
    v = x.var(dim=axis, keepdim=True, unbiased=False)
    return (x - m) / torch.sqrt(v + eps)
def _scatter_add(tgt, idx, src, axis=0):
    return tgt.clone().index_add_(axis, idx, src)
def _segment_sum(src, seg, n):
    out = torch.zeros((n,) + src.shape[1:], dtype=src.dtype)
    return out.index_add_(0, seg, src)
def _pad(x, pw):
    flat = tuple(v for pair in reversed(pw) for v in pair)
    return torch.nn.functional.pad(x, flat)

kernel_name = 'concat_heads__mha_block_cached__gqa_block_cached__near_ridge'

def setup_inputs():

    _w = (lambda a: (lambda t: t.to(torch.bfloat16) if t.is_floating_point() else t)(torch.as_tensor(a)))
    def _A_setup():
        B, Sq, D = 8, 112, 512
        H, Sk, Dh = 32, 512, 128
        return {
            "x": _w((lambda *s: torch.randn(*s, dtype=torch.bfloat16) * 0.8)(B, Sq, D)),
            "k_cache": _w((lambda *s: torch.randn(*s, dtype=torch.bfloat16) * 0.8)(B, H, Sk, Dh)),
            "v_cache": _w((lambda *s: torch.randn(*s, dtype=torch.bfloat16) * 0.05)(B, H, Sk, Dh)),
            "wq": _w((lambda *s: torch.randn(*s, dtype=torch.bfloat16) * 0.8)(D, H * Dh) * (D ** -0.5)),
            "wo": _w((lambda *s: torch.randn(*s, dtype=torch.bfloat16) * 0.05)(H * Dh, D) * ((H * Dh) ** -0.5)),
        }
    _A_in = _A_setup()

    def _B_setup():
        B, Sq, D = 8, 112, 512
        Hq, Hkv, Sk, Dh = 32, 32, 512, 128
        assert Hq % Hkv == 0, (Hq, Hkv)
        return {
            "x": _w((lambda *s: torch.randn(*s, dtype=torch.bfloat16) * 0.8)(B, Sq, D)),
            "k_cache": _w((lambda *s: torch.randn(*s, dtype=torch.bfloat16) * 0.8)(B, Hkv, Sk, Dh)),
            "v_cache": _w((lambda *s: torch.randn(*s, dtype=torch.bfloat16) * 0.05)(B, Hkv, Sk, Dh)),
            "wq": _w((lambda *s: torch.randn(*s, dtype=torch.bfloat16) * 0.8)(D, Hq * Dh) * (D ** -0.5)),
            "wo": _w((lambda *s: torch.randn(*s, dtype=torch.bfloat16) * 0.05)(Hq * Dh, D) * ((Hq * Dh) ** -0.5)),
        }
    _B_in = _B_setup()

    return {
        "A_x": _A_in["x"],
        "A_k_cache": _A_in["k_cache"],
        "A_v_cache": _A_in["v_cache"],
        "A_wq": _A_in["wq"],
        "A_wo": _A_in["wo"],
        "B_x": _B_in["x"],
        "B_k_cache": _B_in["k_cache"],
        "B_v_cache": _B_in["v_cache"],
        "B_wq": _B_in["wq"],
        "B_wo": _B_in["wo"],
    }

def reference(A_x, A_k_cache, A_v_cache, A_wq, A_wo, B_x, B_k_cache, B_v_cache, B_wq, B_wo):
    def _A_ref(x, k_cache, v_cache, wq, wo):
        B, Sq, D = x.shape
        H, Dh = 32, 128
        q = (lambda x, *d: x.permute(*d))((x @ wq).reshape(B, Sq, H, Dh), 0, 2, 1, 3)
        scores = torch.einsum("bhqd,bhkd->bhqk", q, k_cache) * (Dh ** -0.5)
        p = _softmax(scores, axis=-1)
        o = torch.einsum("bhqk,bhkd->bhqd", p, v_cache)
        return (lambda x, *d: x.permute(*d))(o, 0, 2, 1, 3).reshape(B, Sq, H * Dh) @ wo

    def _B_ref(x, k_cache, v_cache, wq, wo):
        B, Sq, D = x.shape
        Hq, Dh = 32, 128
        Hkv = k_cache.shape[1]
        rep = Hq // Hkv
        q = (lambda x, *d: x.permute(*d))((x @ wq).reshape(B, Sq, Hq, Dh), 0, 2, 1, 3)
        k = (lambda x, n, axis: torch.repeat_interleave(x, n, dim=axis))(k_cache, rep, axis=1)
        v = (lambda x, n, axis: torch.repeat_interleave(x, n, dim=axis))(v_cache, rep, axis=1)
        scores = torch.einsum("bhqd,bhkd->bhqk", q, k) * (Dh ** -0.5)
        p = _softmax(scores, axis=-1)
        o = torch.einsum("bhqk,bhkd->bhqd", p, v)
        return (lambda x, *d: x.permute(*d))(o, 0, 2, 1, 3).reshape(B, Sq, Hq * Dh) @ wo

    a = _A_ref(A_x, A_k_cache, A_v_cache, A_wq, A_wo)
    b = _B_ref(B_x, B_k_cache, B_v_cache, B_wq, B_wo)
    return torch.concatenate([a, b], axis=-1)
```

```cpp
#include <hip/hip_runtime.h>
#include <hip/hip_bf16.h>
#include <stdint.h>
#include <utility>

typedef short    bf16x8 __attribute__((ext_vector_type(8)));
typedef short    s16x4  __attribute__((ext_vector_type(4)));
typedef float    f32x16 __attribute__((ext_vector_type(16)));
typedef float    f32x4  __attribute__((ext_vector_type(4)));
typedef float    f32x2  __attribute__((ext_vector_type(2)));
typedef __bf16   bf16x2 __attribute__((ext_vector_type(2)));
typedef unsigned u32x4  __attribute__((ext_vector_type(4)));
typedef __attribute__((address_space(3))) char* lds_ptr;
typedef __attribute__((address_space(3))) s16x4 lds_s16x4;
typedef __attribute__((address_space(3))) bf16x8 lds_bf16x8;
typedef __attribute__((address_space(3))) u32x4 lds_u32x4;

#define DI __device__ __forceinline__
#define MFMA32(a, b, c) __builtin_amdgcn_mfma_f32_32x32x16_bf16((a), (b), (c), 0, 0, 0)
#define SBAR() __builtin_amdgcn_sched_barrier(0)

constexpr int NB = 8, SQ = 112, DM = 512, NH = 32, SK = 512, DH = 128;
constexpr int QG = NB * SQ;
constexpr int TILE = 16384;
constexpr float SCL2 = 0.088388347648318447f * 1.4426950408889634f;
constexpr float RESC_THR = 6.0f;

constexpr size_t WS_O = 0;
constexpr size_t WS_O_BR = (size_t)512 * QG * 16;
constexpr size_t WS_P = 2 * WS_O_BR;
constexpr size_t WS_P_SPLIT = (size_t)2 * 512 * QG * 4;

DI unsigned cvtpk(float lo, float hi) {
  f32x2 v = {lo, hi}; bf16x2 b = __builtin_convertvector(v, bf16x2); return __builtin_bit_cast(unsigned, b);
}
DI bf16x8 pack8(const f32x16& x, int s, float sc) {
  u32x4 w = {cvtpk(x[8 * s + 0] * sc, x[8 * s + 1] * sc), cvtpk(x[8 * s + 2] * sc, x[8 * s + 3] * sc),
             cvtpk(x[8 * s + 4] * sc, x[8 * s + 5] * sc), cvtpk(x[8 * s + 6] * sc, x[8 * s + 7] * sc)};
  return __builtin_bit_cast(bf16x8, w);
}
DI bf16x8 pack8n(const f32x16& x, int s) {
  u32x4 w = {cvtpk(x[8 * s + 0], x[8 * s + 1]), cvtpk(x[8 * s + 2], x[8 * s + 3]),
             cvtpk(x[8 * s + 4], x[8 * s + 5]), cvtpk(x[8 * s + 6], x[8 * s + 7])};
  return __builtin_bit_cast(bf16x8, w);
}
typedef __amdgpu_buffer_rsrc_t srd_t;
DI srd_t make_srd(const void* p, unsigned bytes) {
  return __builtin_amdgcn_make_buffer_rsrc((void*)p, 0, bytes, 0x00020000);
}
DI void dma16(const srd_t& srd, unsigned voff, unsigned soff, unsigned ldsaddr) {
  asm volatile("s_mov_b32 m0, %0\n\ts_nop 0\n\tbuffer_load_dwordx4 %1, %2, %3 offen lds"
               :: "s"(ldsaddr), "v"(voff), "s"(srd), "s"(soff) : "memory");
}
template <int IMM> DI u32x4 bload16(const srd_t& srd, unsigned voff, unsigned soff) {
  u32x4 r;
  asm volatile("buffer_load_dwordx4 %0, %1, %2, %3 offen offset:%4" : "=v"(r) : "v"(voff), "s"(srd), "s"(soff), "i"(IMM) : "memory");
  return r;
}
template <int N> DI void wait_vm() { asm volatile("s_waitcnt vmcnt(%0)" :: "i"(N) : "memory"); }
template <int N> DI void wait_vm4(u32x4& a, u32x4& b, u32x4& c, u32x4& d) {
  asm volatile("s_waitcnt vmcnt(%4)" : "+v"(a), "+v"(b), "+v"(c), "+v"(d) : "i"(N) : "memory");
}
DI void barrier_lds() { asm volatile("s_waitcnt lgkmcnt(0)\n\ts_barrier" ::: "memory"); }
DI s16x4 trread(lds_ptr p) {
  return __builtin_amdgcn_ds_read_tr16_b64_v4i16((lds_s16x4*)p);
}
DI bf16x8 tr_frag(lds_ptr base, int off) {
  s16x4 lo = trread(base + off), hi = trread(base + off + 256);
  return __builtin_shufflevector(lo, hi, 0, 1, 2, 3, 4, 5, 6, 7);
}

template <int... Is> using iseq = std::integer_sequence<int, Is...>;

struct AttnArgs {
  const void* x[2]; const void* k[2]; const void* v[2]; const void* wq[2]; void* o;
};

__host__ __device__ constexpr int tslot(int n) { return n % 5 == 0 ? 3 : n % 5 == 1 ? 4 : n % 5 == 2 ? 2 : n % 5 == 3 ? 0 : 1; }

struct St {
  srd_t sK, sV, sW;
  unsigned lds0, wid;
  unsigned vK0, vK1, vV, vW;
  lds_ptr trb, kb0, kb1;
  bf16x8 qf[8];
  f32x16 o[4];
  float m, l;
};

template <int N> DI void issue_T(St& s) {
  constexpr int slot = tslot(N), j = N >> 1;
  const unsigned ld = s.lds0 + slot * TILE + s.wid * 4096, so = j * TILE + s.wid * 4096;
  if constexpr ((N & 1) == 0) {
    dma16(s.sK, s.vK0, so, ld); dma16(s.sK, s.vK0, so + 128, ld + 1024);
    dma16(s.sK, s.vK1, so + 2048, ld + 2048); dma16(s.sK, s.vK1, so + 2048 + 128, ld + 3072);
  } else {
    dma16(s.sV, s.vV, so, ld); dma16(s.sV, s.vV, so + 128, ld + 1024);
    dma16(s.sV, s.vV, so + 2048, ld + 2048); dma16(s.sV, s.vV, so + 2048 + 128, ld + 3072);
  }
}
template <int N> DI void issue_T_half(St& s) {
  constexpr int slot = tslot(N);
  const unsigned hw = s.wid - 2;
  const unsigned ld = s.lds0 + slot * TILE + hw * 8192, so = hw * 8192;
#pragma unroll
  for (int u = 0; u < 8; ++u) {
    const unsigned o2 = (u >> 1) * 2048 + (u & 1) * 128;
    if constexpr ((N & 1) == 0) dma16(s.sK, ((u >> 1) & 1) ? s.vK1 : s.vK0, so + o2, ld + u * 1024);
    else dma16(s.sV, s.vV, so + o2, ld + u * 1024);
  }
}
template <int I> DI void issue_W(St& s) {
  constexpr int slot = I % 3;
  const unsigned ld = s.lds0 + slot * TILE + s.wid * 8192, so = I * 64 * 8192 + s.wid * 32 * 8192;
#pragma unroll
  for (int u = 0; u < 8; ++u) dma16(s.sW, s.vW, so + (u >> 1) * 8 * 8192 + (u & 1) * 128, ld + u * 1024);
}

template <int I> DI void qproj_step(St& s, f32x16 (&acc)[4], const bf16x8 (&xf)[32]) {
  if (s.wid < 2) { if constexpr (I < 7) wait_vm<8>(); else wait_vm<4>(); }
  barrier_lds();
  if constexpr (I == 0) { if (s.wid >= 2) issue_T_half<1>(s); }
  else if constexpr (I <= 5) { if (s.wid < 2) issue_W<I + 2>(s); }
  else issue_T<I - 4>(s);
  constexpr int sb = (I % 3) * TILE;
#pragma unroll
  for (int ks = 0; ks < 4; ++ks)
#pragma unroll
    for (int dt = 0; dt < 4; ++dt)
      acc[dt] = MFMA32(tr_frag(s.trb, sb + ks * 4096 + dt * 512), xf[4 * I + ks], acc[dt]);
}

template <int J> DI void attn_step(St& s) {
  constexpr int nk = 2 * J, nv = 2 * J + 1;
  if constexpr (nk == 0) wait_vm<16>(); else if constexpr (nk <= 12) wait_vm<12>(); else wait_vm<4>();
  barrier_lds();
  if constexpr (nk + 4 <= 15) issue_T<nk + 4>(s);
  constexpr int ksb = tslot(nk) * TILE;
  f32x16 p0 = {}, p1 = {};
#pragma unroll
  for (int d = 0; d < 8; ++d) {
    lds_ptr b = (d & 1) ? s.kb1 : s.kb0;
    bf16x8 a0 = *(lds_bf16x8*)(b + ksb + (d >> 1) * 512);
    bf16x8 a1 = *(lds_bf16x8*)(b + ksb + 8192 + (d >> 1) * 512);
    p0 = MFMA32(a0, s.qf[d], p0);
    p1 = MFMA32(a1, s.qf[d], p1);
  }
  float mx = fmaxf(fmaxf(p0[0], p0[1]), p1[0]);
  float my = fmaxf(fmaxf(p0[2], p0[3]), p1[1]);
  mx = fmaxf(fmaxf(mx, p1[2]), p1[3]);
#pragma unroll
  for (int r = 4; r < 16; r += 4) {
    mx = fmaxf(fmaxf(mx, p0[r]), p0[r + 1]); my = fmaxf(fmaxf(my, p0[r + 2]), p0[r + 3]);
    mx = fmaxf(fmaxf(mx, p1[r]), p1[r + 1]); my = fmaxf(fmaxf(my, p1[r + 2]), p1[r + 3]);
  }
  mx = fmaxf(mx, my);
  { auto rr = __builtin_amdgcn_permlane32_swap(__float_as_uint(mx), __float_as_uint(mx), false, false);
    mx = fmaxf(__uint_as_float(rr[0]), __uint_as_float(rr[1])); }
  mx *= SCL2;
  if constexpr (J == 0) { s.m = mx; }
  else {
    if (__builtin_expect(__any(mx - s.m > RESC_THR), 0)) {
      const float mn = fmaxf(s.m, mx), f = __builtin_amdgcn_exp2f(s.m - mn);
      s.m = mn; s.l *= f;
#pragma unroll
      for (int dt = 0; dt < 4; ++dt)
#pragma unroll
        for (int r = 0; r < 16; ++r) s.o[dt][r] *= f;
    }
  }
  const float nm = -s.m;
  float la = 0.f, lb = 0.f;
#pragma unroll
  for (int r = 0; r < 16; ++r) {
    p0[r] = __builtin_amdgcn_exp2f(__builtin_fmaf(p0[r], SCL2, nm)); la += p0[r];
    p1[r] = __builtin_amdgcn_exp2f(__builtin_fmaf(p1[r], SCL2, nm)); lb += p1[r];
  }
  s.l += la + lb;
  bf16x8 pf[4] = {pack8n(p0, 0), pack8n(p0, 1), pack8n(p1, 0), pack8n(p1, 1)};
  if constexpr (nv <= 12) wait_vm<12>(); else if constexpr (nv == 13) wait_vm<8>(); else wait_vm<0>();
  barrier_lds();
  if constexpr (nv + 4 <= 15) issue_T<nv + 4>(s);
  constexpr int vsb = tslot(nv) * TILE;
#pragma unroll
  for (int ks = 0; ks < 4; ++ks)
#pragma unroll
    for (int dt = 0; dt < 4; ++dt)
      s.o[dt] = MFMA32(tr_frag(s.trb, vsb + ks * 4096 + dt * 512), pf[ks], s.o[dt]);
}

template <int G> DI void x_stage(u32x4 (&xr)[32], bf16x8 (&xf)[32], lds_ptr wr, lds_ptr rd0) {
  wait_vm4<8 + 28 - 4 * G>(xr[4 * G], xr[4 * G + 1], xr[4 * G + 2], xr[4 * G + 3]);
#pragma unroll
  for (int rr = 0; rr < 4; ++rr) *(lds_u32x4*)(wr + rr * 1024) = xr[4 * G + rr];
#pragma unroll
  for (int ks = 0; ks < 4; ++ks) {
    lds_ptr p = (lds_ptr)((unsigned)(uintptr_t)rd0 ^ (unsigned)(ks * 32));
    xf[4 * G + ks] = *(lds_bf16x8*)p;
  }
}
template <int... Gs> DI void x_stage_all(u32x4 (&xr)[32], bf16x8 (&xf)[32], lds_ptr wr, lds_ptr rd0, iseq<Gs...>) { (x_stage<Gs>(xr, xf, wr, rd0), ...); }
template <int... Ms> DI void x_load_all(u32x4 (&xr)[32], const srd_t& sX, unsigned vx, iseq<Ms...>) {
  ((xr[Ms] = bload16<(Ms >> 2) * 128>(sX, vx, (Ms & 3) * 8192u)), ...);
}
template <int... Is> DI void qproj_all(St& s, f32x16 (&acc)[4], const bf16x8 (&xf)[32], iseq<Is...>) { (qproj_step<Is>(s, acc, xf), ...); }
template <int... Js> DI void attn_all(St& s, iseq<Js...>) { (attn_step<Js>(s), ...); }

__global__ __launch_bounds__(256, 2) void attn_kernel(AttnArgs a) {
  __shared__ __attribute__((aligned(1024))) char smem[5 * TILE];
  const int tid = threadIdx.x, lane = tid & 63;
  const unsigned wid = __builtin_amdgcn_readfirstlane(tid >> 6);
  const int bid = blockIdx.x, g = bid & 7, r = bid >> 3;
  const int br = g >> 2, h = (g & 3) * 8 + (r & 7), b = r >> 3;
  St s;
  s.wid = wid; s.lds0 = (unsigned)(uintptr_t)(lds_ptr)smem;
  const size_t kvoff = ((size_t)(b * NH + h)) * SK * DH * 2;
  s.sK = make_srd((const char*)a.k[br] + kvoff, SK * DH * 2);
  s.sV = make_srd((const char*)a.v[br] + kvoff, SK * DH * 2);
  s.sW = make_srd((const char*)a.wq[br] + h * 256, DM * NH * DH * 2 - h * 256);
  const srd_t sX = make_srd((const char*)a.x[br] + (size_t)b * SQ * DM * 2, SQ * DM * 2);
  {
    const unsigned r8 = (lane >> 2) & 7, cbl = lane >> 5, c4 = lane & 3;
    s.vK0 = r8 * 256 + cbl * 64 + ((c4 ^ (r8 >> 2)) * 16);
    s.vK1 = r8 * 256 + cbl * 64 + ((c4 ^ (2 + (r8 >> 2))) * 16);
    s.vV = r8 * 256 + cbl * 64 + c4 * 16;
    s.vW = r8 * 8192 + cbl * 64 + c4 * 16;
  }
  const int hh = lane >> 5, q32 = lane & 31;
  {
    const int blk = (lane >> 4) & 1, q4 = (lane & 15) >> 2, p = lane & 3, ps = ((p & 1) << 1) | (p >> 1);
    s.trb = (lds_ptr)smem + (hh * 2048 + q4 * 64 + blk * 32 + ps * 8);
    const int rp = (q32 & ~12) | ((q32 & 4) << 1) | ((q32 & 8) >> 1);
    const int kb = 2048 * (rp >> 3) + 64 * (rp & 7) + 16 * (hh ^ ((rp >> 2) & 3));
    s.kb0 = (lds_ptr)smem + kb; s.kb1 = (lds_ptr)smem + (kb ^ 32);
  }
  u32x4 xr[32]; bf16x8 xf[32];
  const unsigned vx = wid * 32768 + (lane >> 3) * 1024 + (lane & 7) * 16;
  if (wid < 2) { issue_W<0>(s); issue_W<1>(s); }
  x_load_all(xr, sX, vx, std::make_integer_sequence<int, 32>{});
  if (wid < 2) issue_W<2>(s); else issue_T_half<0>(s);
  {
    lds_ptr xb = (lds_ptr)smem + 4 * TILE + wid * 4096;
    const int wrow = lane >> 3;
    lds_ptr wr = xb + wrow * 128 + (((lane & 7) ^ wrow) * 16);
    lds_ptr rd0 = xb + q32 * 128 + ((hh ^ (q32 & 7)) * 16);
    x_stage_all(xr, xf, wr, rd0, std::make_integer_sequence<int, 8>{});
  }
  {
    f32x16 acc[4] = {};
    qproj_all(s, acc, xf, std::make_integer_sequence<int, 8>{});
#pragma unroll
    for (int dt = 0; dt < 4; ++dt) { s.qf[2 * dt] = pack8n(acc[dt], 0); s.qf[2 * dt + 1] = pack8n(acc[dt], 1); }
  }
#pragma unroll
  for (int dt = 0; dt < 4; ++dt) s.o[dt] = f32x16{};
  s.l = 0.f; s.m = 0.f;
  attn_all(s, std::make_integer_sequence<int, 8>{});
  float l = s.l;
  { auto rr = __builtin_amdgcn_permlane32_swap(__float_as_uint(l), __float_as_uint(l), false, false);
    l = __uint_as_float(rr[0]) + __uint_as_float(rr[1]); }
  const float inv = 1.0f / l;
  const int q = wid * 32 + q32;
  if (q < SQ) {
    char* ob = (char*)a.o + (size_t)br * WS_O_BR + ((size_t)(h * 16 + hh) * QG + b * SQ + q) * 16;
#pragma unroll
    for (int dt = 0; dt < 4; ++dt)
#pragma unroll
      for (int sgm = 0; sgm < 2; ++sgm)
        *(bf16x8*)(ob + (size_t)(4 * dt + 2 * sgm) * QG * 16) = pack8(s.o[dt], sgm, inv);
  }
}

struct OutArgs { const void* wo[2]; const void* o; void* part; };

struct St2 { srd_t sW, sO; unsigned lds0, wid, vW, vO; lds_ptr trb; f32x16 acc[4]; u32x4 bf[4][4]; };

template <int T> DI void issue2(St2& s) {
  constexpr int slot = T & 3;
  const unsigned ld = s.lds0 + slot * TILE + s.wid * 4096, so = T * 64 * 1024 + s.wid * 16 * 1024;
  dma16(s.sW, s.vW, so, ld); dma16(s.sW, s.vW, so + 128, ld + 1024);
  dma16(s.sW, s.vW, so + 8192, ld + 2048); dma16(s.sW, s.vW, so + 8192 + 128, ld + 3072);
#pragma unroll
  for (int ks = 0; ks < 4; ++ks) s.bf[slot][ks] = bload16<0>(s.sO, s.vO, (unsigned)((8 * T + 2 * ks) * QG * 16));
}
template <int T> DI void out_step(St2& s) {
  constexpr int slot = T & 3;
  constexpr int younger = (T + 2 <= 15 ? 8 : 0) + (T + 1 <= 15 ? 8 : 0);
  wait_vm4<younger>(s.bf[slot][0], s.bf[slot][1], s.bf[slot][2], s.bf[slot][3]);
  barrier_lds();
  if constexpr (T + 3 <= 15) issue2<T + 3>(s);
#pragma unroll
  for (int ks = 0; ks < 4; ++ks)
#pragma unroll
    for (int nt = 0; nt < 4; ++nt)
      s.acc[nt] = MFMA32(tr_frag(s.trb, slot * TILE + ks * 4096 + nt * 512), __builtin_bit_cast(bf16x8, s.bf[slot][ks]), s.acc[nt]);
}
template <int... Ts> DI void out_all(St2& s, iseq<Ts...>) { (out_step<Ts>(s), ...); }

__global__ __launch_bounds__(256, 1) void outproj_kernel(OutArgs a) {
  __shared__ __attribute__((aligned(1024))) char smem[4 * TILE];
  const int tid = threadIdx.x, lane = tid & 63;
  const unsigned wid = __builtin_amdgcn_readfirstlane(tid >> 6);
  const int bid = blockIdx.x, g = bid & 7, r = bid >> 3;
  const int br = g >> 2, sp = g & 3, qt = r >> 2, n4 = r & 3;
  St2 s; s.wid = wid; s.lds0 = (unsigned)(uintptr_t)(lds_ptr)smem;
  s.sW = make_srd((const char*)a.wo[br] + (size_t)sp * 1024 * 1024 + n4 * 256, 1024 * 1024 - n4 * 256);
  s.sO = make_srd((const char*)a.o + br * WS_O_BR + (size_t)sp * 128 * QG * 16, 128 * QG * 16);
  const int hh = lane >> 5, q32 = lane & 31;
  {
    const unsigned r8 = (lane >> 2) & 7, cbl = lane >> 5, c4 = lane & 3;
    s.vW = r8 * 1024 + cbl * 64 + c4 * 16;
    s.vO = (hh * QG + qt * 128 + wid * 32 + q32) * 16;
    const int blk = (lane >> 4) & 1, q4 = (lane & 15) >> 2, p = lane & 3, ps = ((p & 1) << 1) | (p >> 1);
    s.trb = (lds_ptr)smem + (hh * 2048 + q4 * 64 + blk * 32 + ps * 8);
  }
#pragma unroll
  for (int nt = 0; nt < 4; ++nt) s.acc[nt] = f32x16{};
  issue2<0>(s); issue2<1>(s); issue2<2>(s);
  out_all(s, std::make_integer_sequence<int, 16>{});
  float* pb = (float*)((char*)a.part + sp * WS_P_SPLIT) + ((size_t)(br * 64 + n4 * 16 + hh) * QG + qt * 128 + wid * 32 + q32) * 8;
#pragma unroll
  for (int nt = 0; nt < 4; ++nt)
#pragma unroll
    for (int sg = 0; sg < 2; ++sg) {
      float* p = pb + (size_t)(4 * nt + 2 * sg) * QG * 8;
      *(f32x4*)p = f32x4{s.acc[nt][8 * sg], s.acc[nt][8 * sg + 1], s.acc[nt][8 * sg + 2], s.acc[nt][8 * sg + 3]};
      *(f32x4*)(p + 4) = f32x4{s.acc[nt][8 * sg + 4], s.acc[nt][8 * sg + 5], s.acc[nt][8 * sg + 6], s.acc[nt][8 * sg + 7]};
    }
}

__global__ __launch_bounds__(256) void reduce_kernel(const float* __restrict__ part, void* __restrict__ out) {
  const int i = blockIdx.x * 256 + threadIdx.x;
  const int c = i / QG, qg = i - c * QG;
  const float* p = part + (size_t)i * 8;
  f32x4 a0 = *(const f32x4*)p, a1 = *(const f32x4*)(p + 4);
#pragma unroll
  for (int sp = 1; sp < 4; ++sp) {
    const float* pp = (const float*)((const char*)p + sp * WS_P_SPLIT);
    a0 += *(const f32x4*)pp; a1 += *(const f32x4*)(pp + 4);
  }
  u32x4 w = {cvtpk(a0[0], a0[1]), cvtpk(a0[2], a0[3]), cvtpk(a1[0], a1[1]), cvtpk(a1[2], a1[3])};
  *(u32x4*)((char*)out + ((size_t)qg * 1024 + c * 8) * 2) = w;
}

extern "C" void kernel_launch(void* const* d_in, const int* in_sizes, int n_in,
                              void* d_out, int out_size, void* d_ws, size_t ws_size,
                              hipStream_t stream) {
  AttnArgs a;
  a.x[0] = d_in[0]; a.k[0] = d_in[1]; a.v[0] = d_in[2]; a.wq[0] = d_in[3];
  a.x[1] = d_in[5]; a.k[1] = d_in[6]; a.v[1] = d_in[7]; a.wq[1] = d_in[8];
  a.o = (char*)d_ws + WS_O;
  attn_kernel<<<512, 256, 0, stream>>>(a);
  OutArgs o;
  o.wo[0] = d_in[4]; o.wo[1] = d_in[9]; o.o = (char*)d_ws + WS_O; o.part = (char*)d_ws + WS_P;
  outproj_kernel<<<224, 256, 0, stream>>>(o);
  reduce_kernel<<<128 * QG / 256, 256, 0, stream>>>((const float*)((char*)d_ws + WS_P), d_out);
}
```

```cpp
#define KVPOL "sc0 nt"
#include <hip/hip_runtime.h>
#include <hip/hip_bf16.h>
#include <stdint.h>
#include <utility>

typedef short    bf16x8 __attribute__((ext_vector_type(8)));
typedef short    s16x4  __attribute__((ext_vector_type(4)));
typedef float    f32x16 __attribute__((ext_vector_type(16)));
typedef float    f32x4  __attribute__((ext_vector_type(4)));
typedef float    f32x2  __attribute__((ext_vector_type(2)));
typedef __bf16   bf16x2 __attribute__((ext_vector_type(2)));
typedef unsigned u32x4  __attribute__((ext_vector_type(4)));
typedef __attribute__((address_space(3))) char* lds_ptr;
typedef __attribute__((address_space(3))) s16x4 lds_s16x4;
typedef __attribute__((address_space(3))) bf16x8 lds_bf16x8;
typedef __attribute__((address_space(3))) u32x4 lds_u32x4;

#define DI __device__ __forceinline__
#define MFMA32(a, b, c) __builtin_amdgcn_mfma_f32_32x32x16_bf16((a), (b), (c), 0, 0, 0)
#define SBAR() __builtin_amdgcn_sched_barrier(0)

constexpr int NB = 8, SQ = 112, DM = 512, NH = 32, SK = 512, DH = 128;
constexpr int QG = NB * SQ;
constexpr int TILE = 16384;
constexpr float SCL2 = 0.088388347648318447f * 1.4426950408889634f;
constexpr float RESC_THR = 6.0f;

constexpr size_t WS_O = 0;
constexpr size_t WS_O_BR = (size_t)512 * QG * 16;
constexpr size_t WS_P = 2 * WS_O_BR;
constexpr size_t WS_P_SPLIT = (size_t)2 * 512 * QG * 4;

DI unsigned cvtpk(float lo, float hi) {
  f32x2 v = {lo, hi}; bf16x2 b = __builtin_convertvector(v, bf16x2); return __builtin_bit_cast(unsigned, b);
}
DI bf16x8 pack8(const f32x16& x, int s, float sc) {
  u32x4 w = {cvtpk(x[8 * s + 0] * sc, x[8 * s + 1] * sc), cvtpk(x[8 * s + 2] * sc, x[8 * s + 3] * sc),
             cvtpk(x[8 * s + 4] * sc, x[8 * s + 5] * sc), cvtpk(x[8 * s + 6] * sc, x[8 * s + 7] * sc)};
  return __builtin_bit_cast(bf16x8, w);
}
DI bf16x8 pack8n(const f32x16& x, int s) {
  u32x4 w = {cvtpk(x[8 * s + 0], x[8 * s + 1]), cvtpk(x[8 * s + 2], x[8 * s + 3]),
             cvtpk(x[8 * s + 4], x[8 * s + 5]), cvtpk(x[8 * s + 6], x[8 * s + 7])};
  return __builtin_bit_cast(bf16x8, w);
}
typedef __amdgpu_buffer_rsrc_t srd_t;
DI srd_t make_srd(const void* p, unsigned bytes) {
  return __builtin_amdgcn_make_buffer_rsrc((void*)p, 0, bytes, 0x00020000);
}
DI void dma16(const srd_t& srd, unsigned voff, unsigned soff, unsigned ldsaddr) {
  asm volatile("s_mov_b32 m0, %0\n\ts_nop 0\n\tbuffer_load_dwordx4 %1, %2, %3 offen lds"
               :: "s"(ldsaddr), "v"(voff), "s"(srd), "s"(soff) : "memory");
}
DI void dma16s(const srd_t& srd, unsigned voff, unsigned soff, unsigned ldsaddr) {
  asm volatile("s_mov_b32 m0, %0\n\ts_nop 0\n\tbuffer_load_dwordx4 %1, %2, %3 offen " KVPOL " lds"
               :: "s"(ldsaddr), "v"(voff), "s"(srd), "s"(soff) : "memory");
}
template <int IMM> DI u32x4 bload16(const srd_t& srd, unsigned voff, unsigned soff) {
  u32x4 r;
  asm volatile("buffer_load_dwordx4 %0, %1, %2, %3 offen offset:%4" : "=v"(r) : "v"(voff), "s"(srd), "s"(soff), "i"(IMM) : "memory");
  return r;
}
template <int N> DI void wait_vm() { asm volatile("s_waitcnt vmcnt(%0)" :: "i"(N) : "memory"); }
template <int N> DI void wait_vm4(u32x4& a, u32x4& b, u32x4& c, u32x4& d) {
  asm volatile("s_waitcnt vmcnt(%4)" : "+v"(a), "+v"(b), "+v"(c), "+v"(d) : "i"(N) : "memory");
}
DI void barrier_lds() { asm volatile("s_waitcnt lgkmcnt(0)\n\ts_barrier" ::: "memory"); }
DI s16x4 trread(lds_ptr p) {
  return __builtin_amdgcn_ds_read_tr16_b64_v4i16((lds_s16x4*)p);
}
DI bf16x8 tr_frag(lds_ptr base, int off) {
  s16x4 lo = trread(base + off), hi = trread(base + off + 256);
  return __builtin_shufflevector(lo, hi, 0, 1, 2, 3, 4, 5, 6, 7);
}

template <int... Is> using iseq = std::integer_sequence<int, Is...>;

struct AttnArgs {
  const void* x[2]; const void* k[2]; const void* v[2]; const void* wq[2]; void* o;
};

__host__ __device__ constexpr int tslot(int n) { return n % 5 == 0 ? 3 : n % 5 == 1 ? 4 : n % 5 == 2 ? 2 : n % 5 == 3 ? 0 : 1; }

struct St {
  srd_t sK, sV, sW;
  unsigned lds0, wid;
  unsigned vK0, vK1, vV, vW;
  lds_ptr trb, kb0, kb1;
  bf16x8 qf[8];
  f32x16 o[4];
  float m, l;
};

template <int N> DI void issue_T(St& s) {
  constexpr int slot = tslot(N), j = N >> 1;
  const unsigned ld = s.lds0 + slot * TILE + s.wid * 4096, so = j * TILE + s.wid * 4096;
  if constexpr ((N & 1) == 0) {
    dma16s(s.sK, s.vK0, so, ld); dma16s(s.sK, s.vK0, so + 128, ld + 1024);
    dma16s(s.sK, s.vK1, so + 2048, ld + 2048); dma16s(s.sK, s.vK1, so + 2048 + 128, ld + 3072);
  } else {
    dma16s(s.sV, s.vV, so, ld); dma16s(s.sV, s.vV, so + 128, ld + 1024);
    dma16s(s.sV, s.vV, so + 2048, ld + 2048); dma16s(s.sV, s.vV, so + 2048 + 128, ld + 3072);
  }
}
template <int N> DI void issue_T_half(St& s) {
  constexpr int slot = tslot(N);
  const unsigned hw = s.wid - 2;
  const unsigned ld = s.lds0 + slot * TILE + hw * 8192, so = hw * 8192;
#pragma unroll
  for (int u = 0; u < 8; ++u) {
    const unsigned o2 = (u >> 1) * 2048 + (u & 1) * 128;
    if constexpr ((N & 1) == 0) dma16s(s.sK, ((u >> 1) & 1) ? s.vK1 : s.vK0, so + o2, ld + u * 1024);
    else dma16s(s.sV, s.vV, so + o2, ld + u * 1024);
  }
}
template <int I> DI void issue_W(St& s) {
  constexpr int slot = I % 3;
  const unsigned ld = s.lds0 + slot * TILE + s.wid * 8192, so = I * 64 * 8192 + s.wid * 32 * 8192;
#pragma unroll
  for (int u = 0; u < 8; ++u) dma16(s.sW, s.vW, so + (u >> 1) * 8 * 8192 + (u & 1) * 128, ld + u * 1024);
}

template <int I> DI void qproj_step(St& s, f32x16 (&acc)[4], const bf16x8 (&xf)[32]) {
  if (s.wid < 2) { if constexpr (I < 7) wait_vm<8>(); else wait_vm<4>(); }
  barrier_lds();
  if constexpr (I == 0) { if (s.wid >= 2) issue_T_half<1>(s); }
  else if constexpr (I <= 5) { if (s.wid < 2) issue_W<I + 2>(s); }
  else issue_T<I - 4>(s);
  constexpr int sb = (I % 3) * TILE;
#pragma unroll
  for (int ks = 0; ks < 4; ++ks)
#pragma unroll
    for (int dt = 0; dt < 4; ++dt)
      acc[dt] = MFMA32(tr_frag(s.trb, sb + ks * 4096 + dt * 512), xf[4 * I + ks], acc[dt]);
}

template <int J> DI void attn_step(St& s) {
  constexpr int nk = 2 * J, nv = 2 * J + 1;
  if constexpr (nk == 0) wait_vm<16>(); else if constexpr (nk <= 12) wait_vm<12>(); else wait_vm<4>();
  barrier_lds();
  if constexpr (nk + 4 <= 15) issue_T<nk + 4>(s);
  constexpr int ksb = tslot(nk) * TILE;
  f32x16 p0 = {}, p1 = {};
#pragma unroll
  for (int d = 0; d < 8; ++d) {
    lds_ptr b = (d & 1) ? s.kb1 : s.kb0;
    bf16x8 a0 = *(lds_bf16x8*)(b + ksb + (d >> 1) * 512);
    bf16x8 a1 = *(lds_bf16x8*)(b + ksb + 8192 + (d >> 1) * 512);
    p0 = MFMA32(a0, s.qf[d], p0);
    p1 = MFMA32(a1, s.qf[d], p1);
  }
  float mx = fmaxf(fmaxf(p0[0], p0[1]), p1[0]);
  float my = fmaxf(fmaxf(p0[2], p0[3]), p1[1]);
  mx = fmaxf(fmaxf(mx, p1[2]), p1[3]);
#pragma unroll
  for (int r = 4; r < 16; r += 4) {
    mx = fmaxf(fmaxf(mx, p0[r]), p0[r + 1]); my = fmaxf(fmaxf(my, p0[r + 2]), p0[r + 3]);
    mx = fmaxf(fmaxf(mx, p1[r]), p1[r + 1]); my = fmaxf(fmaxf(my, p1[r + 2]), p1[r + 3]);
  }
  mx = fmaxf(mx, my);
  { auto rr = __builtin_amdgcn_permlane32_swap(__float_as_uint(mx), __float_as_uint(mx), false, false);
    mx = fmaxf(__uint_as_float(rr[0]), __uint_as_float(rr[1])); }
  mx *= SCL2;
  if constexpr (J == 0) { s.m = mx; }
  else {
    if (__builtin_expect(__any(mx - s.m > RESC_THR), 0)) {
      const float mn = fmaxf(s.m, mx), f = __builtin_amdgcn_exp2f(s.m - mn);
      s.m = mn; s.l *= f;
#pragma unroll
      for (int dt = 0; dt < 4; ++dt)
#pragma unroll
        for (int r = 0; r < 16; ++r) s.o[dt][r] *= f;
    }
  }
  const float nm = -s.m;
  float la = 0.f, lb = 0.f;
#pragma unroll
  for (int r = 0; r < 16; ++r) {
    p0[r] = __builtin_amdgcn_exp2f(__builtin_fmaf(p0[r], SCL2, nm)); la += p0[r];
    p1[r] = __builtin_amdgcn_exp2f(__builtin_fmaf(p1[r], SCL2, nm)); lb += p1[r];
  }
  s.l += la + lb;
  bf16x8 pf[4] = {pack8n(p0, 0), pack8n(p0, 1), pack8n(p1, 0), pack8n(p1, 1)};
  if constexpr (nv <= 12) wait_vm<12>(); else if constexpr (nv == 13) wait_vm<8>(); else wait_vm<0>();
  barrier_lds();
  if constexpr (nv + 4 <= 15) issue_T<nv + 4>(s);
  constexpr int vsb = tslot(nv) * TILE;
#pragma unroll
  for (int ks = 0; ks < 4; ++ks)
#pragma unroll
    for (int dt = 0; dt < 4; ++dt)
      s.o[dt] = MFMA32(tr_frag(s.trb, vsb + ks * 4096 + dt * 512), pf[ks], s.o[dt]);
}

template <int G> DI void x_stage(u32x4 (&xr)[32], bf16x8 (&xf)[32], lds_ptr wr, lds_ptr rd0) {
  wait_vm4<8 + 28 - 4 * G>(xr[4 * G], xr[4 * G + 1], xr[4 * G + 2], xr[4 * G + 3]);
#pragma unroll
  for (int rr = 0; rr < 4; ++rr) *(lds_u32x4*)(wr + rr * 1024) = xr[4 * G + rr];
#pragma unroll
  for (int ks = 0; ks < 4; ++ks) {
    lds_ptr p = (lds_ptr)((unsigned)(uintptr_t)rd0 ^ (unsigned)(ks * 32));
    xf[4 * G + ks] = *(lds_bf16x8*)p;
  }
}
template <int... Gs> DI void x_stage_all(u32x4 (&xr)[32], bf16x8 (&xf)[32], lds_ptr wr, lds_ptr rd0, iseq<Gs...>) { (x_stage<Gs>(xr, xf, wr, rd0), ...); }
template <int... Ms> DI void x_load_all(u32x4 (&xr)[32], const srd_t& sX, unsigned vx, iseq<Ms...>) {
  ((xr[Ms] = bload16<(Ms >> 2) * 128>(sX, vx, (Ms & 3) * 8192u)), ...);
}
template <int... Is> DI void qproj_all(St& s, f32x16 (&acc)[4], const bf16x8 (&xf)[32], iseq<Is...>) { (qproj_step<Is>(s, acc, xf), ...); }
template <int... Js> DI void attn_all(St& s, iseq<Js...>) { (attn_step<Js>(s), ...); }

__global__ __launch_bounds__(256, 2) void attn_kernel(AttnArgs a) {
  __shared__ __attribute__((aligned(1024))) char smem[5 * TILE];
  const int tid = threadIdx.x, lane = tid & 63;
  const unsigned wid = __builtin_amdgcn_readfirstlane(tid >> 6);
  const int bid = blockIdx.x, g = bid & 7, r = bid >> 3;
  const int br = g >> 2, h = (g & 3) * 8 + (r & 7), b = r >> 3;
  St s;
  s.wid = wid; s.lds0 = (unsigned)(uintptr_t)(lds_ptr)smem;
  const size_t kvoff = ((size_t)(b * NH + h)) * SK * DH * 2;
  s.sK = make_srd((const char*)a.k[br] + kvoff, SK * DH * 2);
  s.sV = make_srd((const char*)a.v[br] + kvoff, SK * DH * 2);
  s.sW = make_srd((const char*)a.wq[br] + h * 256, DM * NH * DH * 2 - h * 256);
  const srd_t sX = make_srd((const char*)a.x[br] + (size_t)b * SQ * DM * 2, SQ * DM * 2);
  {
    const unsigned r8 = (lane >> 2) & 7, cbl = lane >> 5, c4 = lane & 3;
    s.vK0 = r8 * 256 + cbl * 64 + ((c4 ^ (r8 >> 2)) * 16);
    s.vK1 = r8 * 256 + cbl * 64 + ((c4 ^ (2 + (r8 >> 2))) * 16);
    s.vV = r8 * 256 + cbl * 64 + c4 * 16;
    s.vW = r8 * 8192 + cbl * 64 + c4 * 16;
  }
  const int hh = lane >> 5, q32 = lane & 31;
  {
    const int blk = (lane >> 4) & 1, q4 = (lane & 15) >> 2, p = lane & 3, ps = ((p & 1) << 1) | (p >> 1);
    s.trb = (lds_ptr)smem + (hh * 2048 + q4 * 64 + blk * 32 + ps * 8);
    const int rp = (q32 & ~12) | ((q32 & 4) << 1) | ((q32 & 8) >> 1);
    const int kb = 2048 * (rp >> 3) + 64 * (rp & 7) + 16 * (hh ^ ((rp >> 2) & 3));
    s.kb0 = (lds_ptr)smem + kb; s.kb1 = (lds_ptr)smem + (kb ^ 32);
  }
  u32x4 xr[32]; bf16x8 xf[32];
  const unsigned vx = wid * 32768 + (lane >> 3) * 1024 + (lane & 7) * 16;
  if (wid < 2) { issue_W<0>(s); issue_W<1>(s); }
  x_load_all(xr, sX, vx, std::make_integer_sequence<int, 32>{});
  if (wid < 2) issue_W<2>(s); else issue_T_half<0>(s);
  {
    lds_ptr xb = (lds_ptr)smem + 4 * TILE + wid * 4096;
    const int wrow = lane >> 3;
    lds_ptr wr = xb + wrow * 128 + (((lane & 7) ^ wrow) * 16);
    lds_ptr rd0 = xb + q32 * 128 + ((hh ^ (q32 & 7)) * 16);
    x_stage_all(xr, xf, wr, rd0, std::make_integer_sequence<int, 8>{});
  }
  {
    f32x16 acc[4] = {};
    qproj_all(s, acc, xf, std::make_integer_sequence<int, 8>{});
#pragma unroll
    for (int dt = 0; dt < 4; ++dt) { s.qf[2 * dt] = pack8n(acc[dt], 0); s.qf[2 * dt + 1] = pack8n(acc[dt], 1); }
  }
#pragma unroll
  for (int dt = 0; dt < 4; ++dt) s.o[dt] = f32x16{};
  s.l = 0.f; s.m = 0.f;
  attn_all(s, std::make_integer_sequence<int, 8>{});
  float l = s.l;
  { auto rr = __builtin_amdgcn_permlane32_swap(__float_as_uint(l), __float_as_uint(l), false, false);
    l = __uint_as_float(rr[0]) + __uint_as_float(rr[1]); }
  const float inv = 1.0f / l;
  const int q = wid * 32 + q32;
  if (q < SQ) {
    char* ob = (char*)a.o + (size_t)br * WS_O_BR + ((size_t)(h * 16 + hh) * QG + b * SQ + q) * 16;
#pragma unroll
    for (int dt = 0; dt < 4; ++dt)
#pragma unroll
      for (int sgm = 0; sgm < 2; ++sgm)
        *(bf16x8*)(ob + (size_t)(4 * dt + 2 * sgm) * QG * 16) = pack8(s.o[dt], sgm, inv);
  }
}

struct OutArgs { const void* wo[2]; const void* o; void* part; };

struct St2 { srd_t sW, sO; unsigned lds0, wid, vW, vO; lds_ptr trb; f32x16 acc[4]; u32x4 bf[4][4]; };

template <int T> DI void issue2(St2& s) {
  constexpr int slot = T & 3;
  const unsigned ld = s.lds0 + slot * TILE + s.wid * 4096, so = T * 64 * 1024 + s.wid * 16 * 1024;
  dma16(s.sW, s.vW, so, ld); dma16(s.sW, s.vW, so + 128, ld + 1024);
  dma16(s.sW, s.vW, so + 8192, ld + 2048); dma16(s.sW, s.vW, so + 8192 + 128, ld + 3072);
#pragma unroll
  for (int ks = 0; ks < 4; ++ks) s.bf[slot][ks] = bload16<0>(s.sO, s.vO, (unsigned)((8 * T + 2 * ks) * QG * 16));
}
template <int T> DI void out_step(St2& s) {
  constexpr int slot = T & 3;
  constexpr int younger = (T + 2 <= 15 ? 8 : 0) + (T + 1 <= 15 ? 8 : 0);
  wait_vm4<younger>(s.bf[slot][0], s.bf[slot][1], s.bf[slot][2], s.bf[slot][3]);
  barrier_lds();
  if constexpr (T + 3 <= 15) issue2<T + 3>(s);
#pragma unroll
  for (int ks = 0; ks < 4; ++ks)
#pragma unroll
    for (int nt = 0; nt < 4; ++nt)
      s.acc[nt] = MFMA32(tr_frag(s.trb, slot * TILE + ks * 4096 + nt * 512), __builtin_bit_cast(bf16x8, s.bf[slot][ks]), s.acc[nt]);
}
template <int... Ts> DI void out_all(St2& s, iseq<Ts...>) { (out_step<Ts>(s), ...); }

__global__ __launch_bounds__(256, 1) void outproj_kernel(OutArgs a) {
  __shared__ __attribute__((aligned(1024))) char smem[4 * TILE];
  const int tid = threadIdx.x, lane = tid & 63;
  const unsigned wid = __builtin_amdgcn_readfirstlane(tid >> 6);
  const int bid = blockIdx.x, g = bid & 7, r = bid >> 3;
  const int br = g >> 2, sp = g & 3, qt = r >> 2, n4 = r & 3;
  St2 s; s.wid = wid; s.lds0 = (unsigned)(uintptr_t)(lds_ptr)smem;
  s.sW = make_srd((const char*)a.wo[br] + (size_t)sp * 1024 * 1024 + n4 * 256, 1024 * 1024 - n4 * 256);
  s.sO = make_srd((const char*)a.o + br * WS_O_BR + (size_t)sp * 128 * QG * 16, 128 * QG * 16);
  const int hh = lane >> 5, q32 = lane & 31;
  {
    const unsigned r8 = (lane >> 2) & 7, cbl = lane >> 5, c4 = lane & 3;
    s.vW = r8 * 1024 + cbl * 64 + c4 * 16;
    s.vO = (hh * QG + qt * 128 + wid * 32 + q32) * 16;
    const int blk = (lane >> 4) & 1, q4 = (lane & 15) >> 2, p = lane & 3, ps = ((p & 1) << 1) | (p >> 1);
    s.trb = (lds_ptr)smem + (hh * 2048 + q4 * 64 + blk * 32 + ps * 8);
  }
#pragma unroll
  for (int nt = 0; nt < 4; ++nt) s.acc[nt] = f32x16{};
  issue2<0>(s); issue2<1>(s); issue2<2>(s);
  out_all(s, std::make_integer_sequence<int, 16>{});
  float* pb = (float*)((char*)a.part + sp * WS_P_SPLIT) + ((size_t)(br * 64 + n4 * 16 + hh) * QG + qt * 128 + wid * 32 + q32) * 8;
#pragma unroll
  for (int nt = 0; nt < 4; ++nt)
#pragma unroll
    for (int sg = 0; sg < 2; ++sg) {
      float* p = pb + (size_t)(4 * nt + 2 * sg) * QG * 8;
      *(f32x4*)p = f32x4{s.acc[nt][8 * sg], s.acc[nt][8 * sg + 1], s.acc[nt][8 * sg + 2], s.acc[nt][8 * sg + 3]};
      *(f32x4*)(p + 4) = f32x4{s.acc[nt][8 * sg + 4], s.acc[nt][8 * sg + 5], s.acc[nt][8 * sg + 6], s.acc[nt][8 * sg + 7]};
    }
}

__global__ __launch_bounds__(256) void reduce_kernel(const float* __restrict__ part, void* __restrict__ out) {
  const int i = blockIdx.x * 256 + threadIdx.x;
  const int c = i / QG, qg = i - c * QG;
  const float* p = part + (size_t)i * 8;
  f32x4 a0 = *(const f32x4*)p, a1 = *(const f32x4*)(p + 4);
#pragma unroll
  for (int sp = 1; sp < 4; ++sp) {
    const float* pp = (const float*)((const char*)p + sp * WS_P_SPLIT);
    a0 += *(const f32x4*)pp; a1 += *(const f32x4*)(pp + 4);
  }
  u32x4 w = {cvtpk(a0[0], a0[1]), cvtpk(a0[2], a0[3]), cvtpk(a1[0], a1[1]), cvtpk(a1[2], a1[3])};
  *(u32x4*)((char*)out + ((size_t)qg * 1024 + c * 8) * 2) = w;
}

extern "C" void kernel_launch(void* const* d_in, const int* in_sizes, int n_in,
                              void* d_out, int out_size, void* d_ws, size_t ws_size,
                              hipStream_t stream) {
  AttnArgs a;
  a.x[0] = d_in[0]; a.k[0] = d_in[1]; a.v[0] = d_in[2]; a.wq[0] = d_in[3];
  a.x[1] = d_in[5]; a.k[1] = d_in[6]; a.v[1] = d_in[7]; a.wq[1] = d_in[8];
  a.o = (char*)d_ws + WS_O;
  attn_kernel<<<512, 256, 0, stream>>>(a);
  OutArgs o;
  o.wo[0] = d_in[4]; o.wo[1] = d_in[9]; o.o = (char*)d_ws + WS_O; o.part = (char*)d_ws + WS_P;
  outproj_kernel<<<224, 256, 0, stream>>>(o);
  reduce_kernel<<<128 * QG / 256, 256, 0, stream>>>((const float*)((char*)d_ws + WS_P), d_out);
}
```

```cpp
#define METRIC 0
#include <hip/hip_runtime.h>
#include <hip/hip_bf16.h>
#include <stdint.h>
#include <utility>

typedef short    bf16x8 __attribute__((ext_vector_type(8)));
typedef short    s16x4  __attribute__((ext_vector_type(4)));
typedef float    f32x16 __attribute__((ext_vector_type(16)));
typedef float    f32x4  __attribute__((ext_vector_type(4)));
typedef float    f32x2  __attribute__((ext_vector_type(2)));
typedef __bf16   bf16x2 __attribute__((ext_vector_type(2)));
typedef unsigned u32x4  __attribute__((ext_vector_type(4)));
typedef __attribute__((address_space(3))) char* lds_ptr;
typedef __attribute__((address_space(3))) s16x4 lds_s16x4;
typedef __attribute__((address_space(3))) bf16x8 lds_bf16x8;
typedef __attribute__((address_space(3))) u32x4 lds_u32x4;

#define DI __device__ __forceinline__
#define MFMA32(a, b, c) __builtin_amdgcn_mfma_f32_32x32x16_bf16((a), (b), (c), 0, 0, 0)
#define SBAR() __builtin_amdgcn_sched_barrier(0)

constexpr int NB = 8, SQ = 112, DM = 512, NH = 32, SK = 512, DH = 128;
constexpr int QG = NB * SQ;
constexpr int TILE = 16384;
constexpr float SCL2 = 0.088388347648318447f * 1.4426950408889634f;
constexpr float RESC_THR = 6.0f;

constexpr size_t WS_O = 0;
constexpr size_t WS_O_BR = (size_t)512 * QG * 16;
constexpr size_t WS_P = 2 * WS_O_BR;
constexpr size_t WS_P_SPLIT = (size_t)2 * 512 * QG * 4;

DI unsigned cvtpk(float lo, float hi) {
  f32x2 v = {lo, hi}; bf16x2 b = __builtin_convertvector(v, bf16x2); return __builtin_bit_cast(unsigned, b);
}
DI bf16x8 pack8(const f32x16& x, int s, float sc) {
  u32x4 w = {cvtpk(x[8 * s + 0] * sc, x[8 * s + 1] * sc), cvtpk(x[8 * s + 2] * sc, x[8 * s + 3] * sc),
             cvtpk(x[8 * s + 4] * sc, x[8 * s + 5] * sc), cvtpk(x[8 * s + 6] * sc, x[8 * s + 7] * sc)};
  return __builtin_bit_cast(bf16x8, w);
}
DI bf16x8 pack8n(const f32x16& x, int s) {
  u32x4 w = {cvtpk(x[8 * s + 0], x[8 * s + 1]), cvtpk(x[8 * s + 2], x[8 * s + 3]),
             cvtpk(x[8 * s + 4], x[8 * s + 5]), cvtpk(x[8 * s + 6], x[8 * s + 7])};
  return __builtin_bit_cast(bf16x8, w);
}
typedef __amdgpu_buffer_rsrc_t srd_t;
DI srd_t make_srd(const void* p, unsigned bytes) {
  return __builtin_amdgcn_make_buffer_rsrc((void*)p, 0, bytes, 0x00020000);
}
DI void dma16(const srd_t& srd, unsigned voff, unsigned soff, unsigned ldsaddr) {
  asm volatile("s_mov_b32 m0, %0\n\ts_nop 0\n\tbuffer_load_dwordx4 %1, %2, %3 offen lds"
               :: "s"(ldsaddr), "v"(voff), "s"(srd), "s"(soff) : "memory");
}
#define KVPOL "nt"
DI void dma16s(const srd_t& srd, unsigned voff, unsigned soff, unsigned ldsaddr) {
  asm volatile("s_mov_b32 m0, %0\n\ts_nop 0\n\tbuffer_load_dwordx4 %1, %2, %3 offen " KVPOL " lds"
               :: "s"(ldsaddr), "v"(voff), "s"(srd), "s"(soff) : "memory");
}
template <int IMM> DI u32x4 bload16(const srd_t& srd, unsigned voff, unsigned soff) {
  u32x4 r;
  asm volatile("buffer_load_dwordx4 %0, %1, %2, %3 offen offset:%4" : "=v"(r) : "v"(voff), "s"(srd), "s"(soff), "i"(IMM) : "memory");
  return r;
}
template <int N> DI void wait_vm() { asm volatile("s_waitcnt vmcnt(%0)" :: "i"(N) : "memory"); }
template <int N> DI void wait_vm4(u32x4& a, u32x4& b, u32x4& c, u32x4& d) {
  asm volatile("s_waitcnt vmcnt(%4)" : "+v"(a), "+v"(b), "+v"(c), "+v"(d) : "i"(N) : "memory");
}
DI void barrier_lds() { asm volatile("s_waitcnt lgkmcnt(0)\n\ts_barrier" ::: "memory"); }
DI s16x4 trread(lds_ptr p) {
  return __builtin_amdgcn_ds_read_tr16_b64_v4i16((lds_s16x4*)p);
}
DI bf16x8 tr_frag(lds_ptr base, int off) {
  s16x4 lo = trread(base + off), hi = trread(base + off + 256);
  return __builtin_shufflevector(lo, hi, 0, 1, 2, 3, 4, 5, 6, 7);
}

template <int... Is> using iseq = std::integer_sequence<int, Is...>;

struct AttnArgs {
  const void* x[2]; const void* k[2]; const void* v[2]; const void* wq[2]; void* o; unsigned long long* ts;
};
#define STAMP(p, i) do { if (threadIdx.x == 0) (p)[i] = __builtin_amdgcn_s_memrealtime(); } while (0)

__host__ __device__ constexpr int tslot(int n) { return n % 5 == 0 ? 3 : n % 5 == 1 ? 4 : n % 5 == 2 ? 2 : n % 5 == 3 ? 0 : 1; }

struct St {
  srd_t sK, sV, sW;
  unsigned lds0, wid;
  unsigned vK0, vK1, vV, vW;
  lds_ptr trb, kb0, kb1;
  bf16x8 qf[8];
  f32x16 o[4];
  float m, l;
};

template <int N> DI void issue_T(St& s) {
  constexpr int slot = tslot(N), j = N >> 1;
  const unsigned ld = s.lds0 + slot * TILE + s.wid * 4096, so = j * TILE + s.wid * 4096;
  if constexpr ((N & 1) == 0) {
    dma16s(s.sK, s.vK0, so, ld); dma16s(s.sK, s.vK0, so + 128, ld + 1024);
    dma16s(s.sK, s.vK1, so + 2048, ld + 2048); dma16s(s.sK, s.vK1, so + 2048 + 128, ld + 3072);
  } else {
    dma16s(s.sV, s.vV, so, ld); dma16s(s.sV, s.vV, so + 128, ld + 1024);
    dma16s(s.sV, s.vV, so + 2048, ld + 2048); dma16s(s.sV, s.vV, so + 2048 + 128, ld + 3072);
  }
}
template <int N> DI void issue_T_half(St& s) {
  constexpr int slot = tslot(N);
  const unsigned hw = s.wid - 2;
  const unsigned ld = s.lds0 + slot * TILE + hw * 8192, so = hw * 8192;
#pragma unroll
  for (int u = 0; u < 8; ++u) {
    const unsigned o2 = (u >> 1) * 2048 + (u & 1) * 128;
    if constexpr ((N & 1) == 0) dma16s(s.sK, ((u >> 1) & 1) ? s.vK1 : s.vK0, so + o2, ld + u * 1024);
    else dma16s(s.sV, s.vV, so + o2, ld + u * 1024);
  }
}
template <int I> DI void issue_W(St& s) {
  constexpr int slot = I % 3;
  const unsigned ld = s.lds0 + slot * TILE + s.wid * 8192, so = I * 64 * 8192 + s.wid * 32 * 8192;
#pragma unroll
  for (int u = 0; u < 8; ++u) dma16(s.sW, s.vW, so + (u >> 1) * 8 * 8192 + (u & 1) * 128, ld + u * 1024);
}

template <int I> DI void qproj_step(St& s, f32x16 (&acc)[4], const bf16x8 (&xf)[32]) {
  if (s.wid < 2) { if constexpr (I < 7) wait_vm<8>(); else wait_vm<4>(); }
  barrier_lds();
  if constexpr (I == 0) { if (s.wid >= 2) issue_T_half<1>(s); }
  else if constexpr (I <= 5) { if (s.wid < 2) issue_W<I + 2>(s); }
  else issue_T<I - 4>(s);
  constexpr int sb = (I % 3) * TILE;
#pragma unroll
  for (int ks = 0; ks < 4; ++ks)
#pragma unroll
    for (int dt = 0; dt < 4; ++dt)
      acc[dt] = MFMA32(tr_frag(s.trb, sb + ks * 4096 + dt * 512), xf[4 * I + ks], acc[dt]);
}

template <int J> DI void attn_step(St& s) {
  constexpr int nk = 2 * J, nv = 2 * J + 1;
  if constexpr (nk == 0) wait_vm<16>(); else if constexpr (nk <= 12) wait_vm<12>(); else wait_vm<4>();
  barrier_lds();
  if constexpr (nk + 4 <= 15) issue_T<nk + 4>(s);
  constexpr int ksb = tslot(nk) * TILE;
  f32x16 p0 = {}, p1 = {};
#pragma unroll
  for (int d = 0; d < 8; ++d) {
    lds_ptr b = (d & 1) ? s.kb1 : s.kb0;
    bf16x8 a0 = *(lds_bf16x8*)(b + ksb + (d >> 1) * 512);
    bf16x8 a1 = *(lds_bf16x8*)(b + ksb + 8192 + (d >> 1) * 512);
    p0 = MFMA32(a0, s.qf[d], p0);
    p1 = MFMA32(a1, s.qf[d], p1);
  }
  float mx = fmaxf(fmaxf(p0[0], p0[1]), p1[0]);
  float my = fmaxf(fmaxf(p0[2], p0[3]), p1[1]);
  mx = fmaxf(fmaxf(mx, p1[2]), p1[3]);
#pragma unroll
  for (int r = 4; r < 16; r += 4) {
    mx = fmaxf(fmaxf(mx, p0[r]), p0[r + 1]); my = fmaxf(fmaxf(my, p0[r + 2]), p0[r + 3]);
    mx = fmaxf(fmaxf(mx, p1[r]), p1[r + 1]); my = fmaxf(fmaxf(my, p1[r + 2]), p1[r + 3]);
  }
  mx = fmaxf(mx, my);
  { auto rr = __builtin_amdgcn_permlane32_swap(__float_as_uint(mx), __float_as_uint(mx), false, false);
    mx = fmaxf(__uint_as_float(rr[0]), __uint_as_float(rr[1])); }
  mx *= SCL2;
  if constexpr (J == 0) { s.m = mx; }
  else {
    if (__builtin_expect(__any(mx - s.m > RESC_THR), 0)) {
      const float mn = fmaxf(s.m, mx), f = __builtin_amdgcn_exp2f(s.m - mn);
      s.m = mn; s.l *= f;
#pragma unroll
      for (int dt = 0; dt < 4; ++dt)
#pragma unroll
        for (int r = 0; r < 16; ++r) s.o[dt][r] *= f;
    }
  }
  const float nm = -s.m;
  float la = 0.f, lb = 0.f;
#pragma unroll
  for (int r = 0; r < 16; ++r) {
    p0[r] = __builtin_amdgcn_exp2f(__builtin_fmaf(p0[r], SCL2, nm)); la += p0[r];
    p1[r] = __builtin_amdgcn_exp2f(__builtin_fmaf(p1[r], SCL2, nm)); lb += p1[r];
  }
  s.l += la + lb;
  bf16x8 pf[4] = {pack8n(p0, 0), pack8n(p0, 1), pack8n(p1, 0), pack8n(p1, 1)};
  if constexpr (nv <= 12) wait_vm<12>(); else if constexpr (nv == 13) wait_vm<8>(); else wait_vm<0>();
  barrier_lds();
  if constexpr (nv + 4 <= 15) issue_T<nv + 4>(s);
  constexpr int vsb = tslot(nv) * TILE;
#pragma unroll
  for (int ks = 0; ks < 4; ++ks)
#pragma unroll
    for (int dt = 0; dt < 4; ++dt)
      s.o[dt] = MFMA32(tr_frag(s.trb, vsb + ks * 4096 + dt * 512), pf[ks], s.o[dt]);
}

template <int G> DI void x_stage(u32x4 (&xr)[32], bf16x8 (&xf)[32], lds_ptr wr, lds_ptr rd0) {
  wait_vm4<8 + 28 - 4 * G>(xr[4 * G], xr[4 * G + 1], xr[4 * G + 2], xr[4 * G + 3]);
#pragma unroll
  for (int rr = 0; rr < 4; ++rr) *(lds_u32x4*)(wr + rr * 1024) = xr[4 * G + rr];
#pragma unroll
  for (int ks = 0; ks < 4; ++ks) {
    lds_ptr p = (lds_ptr)((unsigned)(uintptr_t)rd0 ^ (unsigned)(ks * 32));
    xf[4 * G + ks] = *(lds_bf16x8*)p;
  }
}
template <int... Gs> DI void x_stage_all(u32x4 (&xr)[32], bf16x8 (&xf)[32], lds_ptr wr, lds_ptr rd0, iseq<Gs...>) { (x_stage<Gs>(xr, xf, wr, rd0), ...); }
template <int... Ms> DI void x_load_all(u32x4 (&xr)[32], const srd_t& sX, unsigned vx, iseq<Ms...>) {
  ((xr[Ms] = bload16<(Ms >> 2) * 128>(sX, vx, (Ms & 3) * 8192u)), ...);
}
template <int... Is> DI void qproj_all(St& s, f32x16 (&acc)[4], const bf16x8 (&xf)[32], iseq<Is...>) { (qproj_step<Is>(s, acc, xf), ...); }
template <int... Js> DI void attn_all(St& s, iseq<Js...>) { (attn_step<Js>(s), ...); }

__global__ __launch_bounds__(256, 2) void attn_kernel(AttnArgs a) {
  __shared__ __attribute__((aligned(1024))) char smem[5 * TILE];
  const unsigned long long t_start = __builtin_amdgcn_s_memrealtime();
  const int tid = threadIdx.x, lane = tid & 63;
  const unsigned wid = __builtin_amdgcn_readfirstlane(tid >> 6);
  const int bid = blockIdx.x, g = bid & 7, r = bid >> 3;
  const int br = g >> 2, h = (g & 3) * 8 + (r & 7), b = r >> 3;
  St s;
  s.wid = wid; s.lds0 = (unsigned)(uintptr_t)(lds_ptr)smem;
  const size_t kvoff = ((size_t)(b * NH + h)) * SK * DH * 2;
  s.sK = make_srd((const char*)a.k[br] + kvoff, SK * DH * 2);
  s.sV = make_srd((const char*)a.v[br] + kvoff, SK * DH * 2);
  s.sW = make_srd((const char*)a.wq[br] + h * 256, DM * NH * DH * 2 - h * 256);
  const srd_t sX = make_srd((const char*)a.x[br] + (size_t)b * SQ * DM * 2, SQ * DM * 2);
  {
    const unsigned r8 = (lane >> 2) & 7, cbl = lane >> 5, c4 = lane & 3;
    s.vK0 = r8 * 256 + cbl * 64 + ((c4 ^ (r8 >> 2)) * 16);
    s.vK1 = r8 * 256 + cbl * 64 + ((c4 ^ (2 + (r8 >> 2))) * 16);
    s.vV = r8 * 256 + cbl * 64 + c4 * 16;
    s.vW = r8 * 8192 + cbl * 64 + c4 * 16;
  }
  const int hh = lane >> 5, q32 = lane & 31;
  {
    const int blk = (lane >> 4) & 1, q4 = (lane & 15) >> 2, p = lane & 3, ps = ((p & 1) << 1) | (p >> 1);
    s.trb = (lds_ptr)smem + (hh * 2048 + q4 * 64 + blk * 32 + ps * 8);
    const int rp = (q32 & ~12) | ((q32 & 4) << 1) | ((q32 & 8) >> 1);
    const int kb = 2048 * (rp >> 3) + 64 * (rp & 7) + 16 * (hh ^ ((rp >> 2) & 3));
    s.kb0 = (lds_ptr)smem + kb; s.kb1 = (lds_ptr)smem + (kb ^ 32);
  }
  u32x4 xr[32]; bf16x8 xf[32];
  const unsigned vx = wid * 32768 + (lane >> 3) * 1024 + (lane & 7) * 16;
  if (wid < 2) { issue_W<0>(s); issue_W<1>(s); }
  x_load_all(xr, sX, vx, std::make_integer_sequence<int, 32>{});
  if (wid < 2) issue_W<2>(s); else issue_T_half<0>(s);
  {
    lds_ptr xb = (lds_ptr)smem + 4 * TILE + wid * 4096;
    const int wrow = lane >> 3;
    lds_ptr wr = xb + wrow * 128 + (((lane & 7) ^ wrow) * 16);
    lds_ptr rd0 = xb + q32 * 128 + ((hh ^ (q32 & 7)) * 16);
    x_stage_all(xr, xf, wr, rd0, std::make_integer_sequence<int, 8>{});
  }
  {
    f32x16 acc[4] = {};
    qproj_all(s, acc, xf, std::make_integer_sequence<int, 8>{});
#pragma unroll
    for (int dt = 0; dt < 4; ++dt) { s.qf[2 * dt] = pack8n(acc[dt], 0); s.qf[2 * dt + 1] = pack8n(acc[dt], 1); }
  }
#pragma unroll
  for (int dt = 0; dt < 4; ++dt) s.o[dt] = f32x16{};
  s.l = 0.f; s.m = 0.f;
  attn_all(s, std::make_integer_sequence<int, 8>{});
  float l = s.l;
  { auto rr = __builtin_amdgcn_permlane32_swap(__float_as_uint(l), __float_as_uint(l), false, false);
    l = __uint_as_float(rr[0]) + __uint_as_float(rr[1]); }
  const float inv = 1.0f / l;
  const int q = wid * 32 + q32;
  if (q < SQ) {
    char* ob = (char*)a.o + (size_t)br * WS_O_BR + ((size_t)(h * 16 + hh) * QG + b * SQ + q) * 16;
#pragma unroll
    for (int dt = 0; dt < 4; ++dt)
#pragma unroll
      for (int sgm = 0; sgm < 2; ++sgm)
        *(bf16x8*)(ob + (size_t)(4 * dt + 2 * sgm) * QG * 16) = pack8(s.o[dt], sgm, inv);
  }
  if (threadIdx.x == 0) { a.ts[2 * blockIdx.x] = t_start; a.ts[2 * blockIdx.x + 1] = __builtin_amdgcn_s_memrealtime(); }
}

struct OutArgs { const void* wo[2]; const void* o; void* part; unsigned long long* ts; };

struct St2 { srd_t sW, sO; unsigned lds0, wid, vW, vO; lds_ptr trb; f32x16 acc[4]; u32x4 bf[4][4]; };

template <int T> DI void issue2(St2& s) {
  constexpr int slot = T & 3;
  const unsigned ld = s.lds0 + slot * TILE + s.wid * 4096, so = T * 64 * 1024 + s.wid * 16 * 1024;
  dma16(s.sW, s.vW, so, ld); dma16(s.sW, s.vW, so + 128, ld + 1024);
  dma16(s.sW, s.vW, so + 8192, ld + 2048); dma16(s.sW, s.vW, so + 8192 + 128, ld + 3072);
#pragma unroll
  for (int ks = 0; ks < 4; ++ks) s.bf[slot][ks] = bload16<0>(s.sO, s.vO, (unsigned)((8 * T + 2 * ks) * QG * 16));
}
template <int T> DI void out_step(St2& s) {
  constexpr int slot = T & 3;
  constexpr int younger = (T + 2 <= 15 ? 8 : 0) + (T + 1 <= 15 ? 8 : 0);
  wait_vm4<younger>(s.bf[slot][0], s.bf[slot][1], s.bf[slot][2], s.bf[slot][3]);
  barrier_lds();
  if constexpr (T + 3 <= 15) issue2<T + 3>(s);
#pragma unroll
  for (int ks = 0; ks < 4; ++ks)
#pragma unroll
    for (int nt = 0; nt < 4; ++nt)
      s.acc[nt] = MFMA32(tr_frag(s.trb, slot * TILE + ks * 4096 + nt * 512), __builtin_bit_cast(bf16x8, s.bf[slot][ks]), s.acc[nt]);
}
template <int... Ts> DI void out_all(St2& s, iseq<Ts...>) { (out_step<Ts>(s), ...); }

__global__ __launch_bounds__(256, 1) void outproj_kernel(OutArgs a) {
  __shared__ __attribute__((aligned(1024))) char smem[4 * TILE];
  const unsigned long long t_start = __builtin_amdgcn_s_memrealtime();
  const int tid = threadIdx.x, lane = tid & 63;
  const unsigned wid = __builtin_amdgcn_readfirstlane(tid >> 6);
  const int bid = blockIdx.x, g = bid & 7, r = bid >> 3;
  const int br = g >> 2, sp = g & 3, qt = r >> 2, n4 = r & 3;
  St2 s; s.wid = wid; s.lds0 = (unsigned)(uintptr_t)(lds_ptr)smem;
  s.sW = make_srd((const char*)a.wo[br] + (size_t)sp * 1024 * 1024 + n4 * 256, 1024 * 1024 - n4 * 256);
  s.sO = make_srd((const char*)a.o + br * WS_O_BR + (size_t)sp * 128 * QG * 16, 128 * QG * 16);
  const int hh = lane >> 5, q32 = lane & 31;
  {
    const unsigned r8 = (lane >> 2) & 7, cbl = lane >> 5, c4 = lane & 3;
    s.vW = r8 * 1024 + cbl * 64 + c4 * 16;
    s.vO = (hh * QG + qt * 128 + wid * 32 + q32) * 16;
    const int blk = (lane >> 4) & 1, q4 = (lane & 15) >> 2, p = lane & 3, ps = ((p & 1) << 1) | (p >> 1);
    s.trb = (lds_ptr)smem + (hh * 2048 + q4 * 64 + blk * 32 + ps * 8);
  }
#pragma unroll
  for (int nt = 0; nt < 4; ++nt) s.acc[nt] = f32x16{};
  issue2<0>(s); issue2<1>(s); issue2<2>(s);
  out_all(s, std::make_integer_sequence<int, 16>{});
  float* pb = (float*)((char*)a.part + sp * WS_P_SPLIT) + ((size_t)(br * 64 + n4 * 16 + hh) * QG + qt * 128 + wid * 32 + q32) * 8;
#pragma unroll
  for (int nt = 0; nt < 4; ++nt)
#pragma unroll
    for (int sg = 0; sg < 2; ++sg) {
      float* p = pb + (size_t)(4 * nt + 2 * sg) * QG * 8;
      *(f32x4*)p = f32x4{s.acc[nt][8 * sg], s.acc[nt][8 * sg + 1], s.acc[nt][8 * sg + 2], s.acc[nt][8 * sg + 3]};
      *(f32x4*)(p + 4) = f32x4{s.acc[nt][8 * sg + 4], s.acc[nt][8 * sg + 5], s.acc[nt][8 * sg + 6], s.acc[nt][8 * sg + 7]};
    }
  if (threadIdx.x == 0) { a.ts[2 * blockIdx.x] = t_start; a.ts[2 * blockIdx.x + 1] = __builtin_amdgcn_s_memrealtime(); }
}

__global__ __launch_bounds__(256) void reduce_kernel(const float* __restrict__ part, void* __restrict__ out, unsigned long long* ts) {
  const unsigned long long t_start = __builtin_amdgcn_s_memrealtime();
  const int i = blockIdx.x * 256 + threadIdx.x;
  const int c = i / QG, qg = i - c * QG;
  const float* p = part + (size_t)i * 8;
  f32x4 a0 = *(const f32x4*)p, a1 = *(const f32x4*)(p + 4);
#pragma unroll
  for (int sp = 1; sp < 4; ++sp) {
    const float* pp = (const float*)((const char*)p + sp * WS_P_SPLIT);
    a0 += *(const f32x4*)pp; a1 += *(const f32x4*)(pp + 4);
  }
  u32x4 w = {cvtpk(a0[0], a0[1]), cvtpk(a0[2], a0[3]), cvtpk(a1[0], a1[1]), cvtpk(a1[2], a1[3])};
  *(u32x4*)((char*)out + ((size_t)qg * 1024 + c * 8) * 2) = w;
  if (threadIdx.x == 0) { ts[2 * blockIdx.x] = t_start; ts[2 * blockIdx.x + 1] = __builtin_amdgcn_s_memrealtime(); }
}
__global__ void spin_kernel(const unsigned long long* ts) {
  unsigned long long mn[3], mx[3]; const int nb[3] = {512, 224, 448};
  for (int k = 0; k < 3; ++k) { mn[k] = ~0ull; mx[k] = 0; for (int i = 0; i < nb[k]; ++i) { unsigned long long a = ts[k * 2048 + 2 * i], b = ts[k * 2048 + 2 * i + 1]; mn[k] = a < mn[k] ? a : mn[k]; mx[k] = b > mx[k] ? b : mx[k]; } }
  long long x = 0;
  if (METRIC == 1) x = mx[0] - mn[0]; if (METRIC == 2) x = mn[1] - mx[0]; if (METRIC == 3) x = mx[1] - mn[1]; if (METRIC == 4) x = mn[2] - mx[1]; if (METRIC == 5) x = mx[2] - mn[2];
  if (x < 0) x = 0; if (x > 20000) x = 20000;
  const unsigned long long t0 = __builtin_amdgcn_s_memrealtime();
  while (__builtin_amdgcn_s_memrealtime() - t0 < (unsigned long long)(10 * x)) __builtin_amdgcn_s_sleep(1);
}

extern "C" void kernel_launch(void* const* d_in, const int* in_sizes, int n_in,
                              void* d_out, int out_size, void* d_ws, size_t ws_size,
                              hipStream_t stream) {
  AttnArgs a;
  a.x[0] = d_in[0]; a.k[0] = d_in[1]; a.v[0] = d_in[2]; a.wq[0] = d_in[3];
  a.x[1] = d_in[5]; a.k[1] = d_in[6]; a.v[1] = d_in[7]; a.wq[1] = d_in[8];
  a.o = (char*)d_ws + WS_O; unsigned long long* ts = (unsigned long long*)((char*)d_ws + (64u << 20)); a.ts = ts;
  attn_kernel<<<512, 256, 0, stream>>>(a);
  OutArgs o;
  o.wo[0] = d_in[4]; o.wo[1] = d_in[9]; o.o = (char*)d_ws + WS_O; o.part = (char*)d_ws + WS_P; o.ts = ts + 2048;
  outproj_kernel<<<224, 256, 0, stream>>>(o);
  reduce_kernel<<<128 * QG / 256, 256, 0, stream>>>((const float*)((char*)d_ws + WS_P), d_out, ts + 4096);
  spin_kernel<<<1, 64, 0, stream>>>(ts);
}
```

```cpp
#define METRIC 0
#include <hip/hip_runtime.h>
#include <hip/hip_bf16.h>
#include <stdint.h>
#include <utility>

typedef short    bf16x8 __attribute__((ext_vector_type(8)));
typedef short    s16x4  __attribute__((ext_vector_type(4)));
typedef float    f32x16 __attribute__((ext_vector_type(16)));
typedef float    f32x4  __attribute__((ext_vector_type(4)));
typedef float    f32x2  __attribute__((ext_vector_type(2)));
typedef __bf16   bf16x2 __attribute__((ext_vector_type(2)));
typedef unsigned u32x4  __attribute__((ext_vector_type(4)));
typedef __attribute__((address_space(3))) char* lds_ptr;
typedef __attribute__((address_space(3))) s16x4 lds_s16x4;
typedef __attribute__((address_space(3))) bf16x8 lds_bf16x8;
typedef __attribute__((address_space(3))) u32x4 lds_u32x4;

#define DI __device__ __forceinline__
#define MFMA32(a, b, c) __builtin_amdgcn_mfma_f32_32x32x16_bf16((a), (b), (c), 0, 0, 0)
#define SBAR() __builtin_amdgcn_sched_barrier(0)

constexpr int NB = 8, SQ = 112, DM = 512, NH = 32, SK = 512, DH = 128;
constexpr int QG = NB * SQ;
constexpr int TILE = 16384;
constexpr float SCL2 = 0.088388347648318447f * 1.4426950408889634f;
constexpr float RESC_THR = 6.0f;

constexpr size_t WS_O = 0;
constexpr size_t WS_O_BR = (size_t)512 * QG * 16;
constexpr size_t WS_P = 2 * WS_O_BR;
constexpr size_t WS_P_SPLIT = (size_t)2 * 512 * QG * 4;

DI unsigned cvtpk(float lo, float hi) {
  f32x2 v = {lo, hi}; bf16x2 b = __builtin_convertvector(v, bf16x2); return __builtin_bit_cast(unsigned, b);
}
DI bf16x8 pack8(const f32x16& x, int s, float sc) {
  u32x4 w = {cvtpk(x[8 * s + 0] * sc, x[8 * s + 1] * sc), cvtpk(x[8 * s + 2] * sc, x[8 * s + 3] * sc),
             cvtpk(x[8 * s + 4] * sc, x[8 * s + 5] * sc), cvtpk(x[8 * s + 6] * sc, x[8 * s + 7] * sc)};
  return __builtin_bit_cast(bf16x8, w);
}
DI bf16x8 pack8n(const f32x16& x, int s) {
  u32x4 w = {cvtpk(x[8 * s + 0], x[8 * s + 1]), cvtpk(x[8 * s + 2], x[8 * s + 3]),
             cvtpk(x[8 * s + 4], x[8 * s + 5]), cvtpk(x[8 * s + 6], x[8 * s + 7])};
  return __builtin_bit_cast(bf16x8, w);
}
typedef __amdgpu_buffer_rsrc_t srd_t;
DI srd_t make_srd(const void* p, unsigned bytes) {
  return __builtin_amdgcn_make_buffer_rsrc((void*)p, 0, bytes, 0x00020000);
}
DI void dma16(const srd_t& srd, unsigned voff, unsigned soff, unsigned ldsaddr) {
  asm volatile("s_mov_b32 m0, %0\n\ts_nop 0\n\tbuffer_load_dwordx4 %1, %2, %3 offen lds"
               :: "s"(ldsaddr), "v"(voff), "s"(srd), "s"(soff) : "memory");
}
#define KVPOL "nt"
DI void dma16s(const srd_t& srd, unsigned voff, unsigned soff, unsigned ldsaddr) {
  asm volatile("s_mov_b32 m0, %0\n\ts_nop 0\n\tbuffer_load_dwordx4 %1, %2, %3 offen " KVPOL " lds"
               :: "s"(ldsaddr), "v"(voff), "s"(srd), "s"(soff) : "memory");
}
template <int IMM> DI u32x4 bload16(const srd_t& srd, unsigned voff, unsigned soff) {
  u32x4 r;
  asm volatile("buffer_load_dwordx4 %0, %1, %2, %3 offen offset:%4" : "=v"(r) : "v"(voff), "s"(srd), "s"(soff), "i"(IMM) : "memory");
  return r;
}
template <int N> DI void wait_vm() { asm volatile("s_waitcnt vmcnt(%0)" :: "i"(N) : "memory"); }
template <int N> DI void wait_vm4(u32x4& a, u32x4& b, u32x4& c, u32x4& d) {
  asm volatile("s_waitcnt vmcnt(%4)" : "+v"(a), "+v"(b), "+v"(c), "+v"(d) : "i"(N) : "memory");
}
DI void barrier_lds() { asm volatile("s_waitcnt lgkmcnt(0)\n\ts_barrier" ::: "memory"); }
DI s16x4 trread(lds_ptr p) {
  return __builtin_amdgcn_ds_read_tr16_b64_v4i16((lds_s16x4*)p);
}
DI bf16x8 tr_frag(lds_ptr base, int off) {
  s16x4 lo = trread(base + off), hi = trread(base + off + 256);
  return __builtin_shufflevector(lo, hi, 0, 1, 2, 3, 4, 5, 6, 7);
}

template <int... Is> using iseq = std::integer_sequence<int, Is...>;

struct AttnArgs {
  const void* x[2]; const void* k[2]; const void* v[2]; const void* wq[2]; void* o; unsigned long long* ts;
};
#define STAMP(p, i) do { if (threadIdx.x == 0) (p)[i] = __builtin_amdgcn_s_memrealtime(); } while (0)

__host__ __device__ constexpr int tslot(int n) { return n % 5 == 0 ? 3 : n % 5 == 1 ? 4 : n % 5 == 2 ? 2 : n % 5 == 3 ? 0 : 1; }

struct St {
  srd_t sK, sV, sW;
  unsigned lds0, wid;
  unsigned vK0, vK1, vV, vW;
  lds_ptr trb, kb0, kb1;
  bf16x8 qf[8];
  f32x16 o[4];
  float m, l;
};

template <int N> DI void issue_T(St& s) {
  constexpr int slot = tslot(N), j = N >> 1;
  const unsigned ld = s.lds0 + slot * TILE + s.wid * 4096, so = j * TILE + s.wid * 4096;
  if constexpr ((N & 1) == 0) {
    dma16s(s.sK, s.vK0, so, ld); dma16s(s.sK, s.vK0, so + 128, ld + 1024);
    dma16s(s.sK, s.vK1, so + 2048, ld + 2048); dma16s(s.sK, s.vK1, so + 2048 + 128, ld + 3072);
  } else {
    dma16s(s.sV, s.vV, so, ld); dma16s(s.sV, s.vV, so + 128, ld + 1024);
    dma16s(s.sV, s.vV, so + 2048, ld + 2048); dma16s(s.sV, s.vV, so + 2048 + 128, ld + 3072);
  }
}
template <int N> DI void issue_T_half(St& s) {
  constexpr int slot = tslot(N);
  const unsigned hw = s.wid - 2;
  const unsigned ld = s.lds0 + slot * TILE + hw * 8192, so = hw * 8192;
#pragma unroll
  for (int u = 0; u < 8; ++u) {
    const unsigned o2 = (u >> 1) * 2048 + (u & 1) * 128;
    if constexpr ((N & 1) == 0) dma16s(s.sK, ((u >> 1) & 1) ? s.vK1 : s.vK0, so + o2, ld + u * 1024);
    else dma16s(s.sV, s.vV, so + o2, ld + u * 1024);
  }
}
template <int I> DI void issue_W(St& s) {
  constexpr int slot = I % 3;
  const unsigned ld = s.lds0 + slot * TILE + s.wid * 8192, so = I * 64 * 8192 + s.wid * 32 * 8192;
#pragma unroll
  for (int u = 0; u < 8; ++u) dma16(s.sW, s.vW, so + (u >> 1) * 8 * 8192 + (u & 1) * 128, ld + u * 1024);
}

template <int I> DI void qproj_step(St& s, f32x16 (&acc)[4], const bf16x8 (&xf)[32]) {
  if (s.wid < 2) { if constexpr (I < 7) wait_vm<8>(); else wait_vm<4>(); }
  barrier_lds();
  if constexpr (I == 0) { if (s.wid >= 2) issue_T_half<1>(s); }
  else if constexpr (I <= 5) { if (s.wid < 2) issue_W<I + 2>(s); }
  else issue_T<I - 4>(s);
  constexpr int sb = (I % 3) * TILE;
#pragma unroll
  for (int ks = 0; ks < 4; ++ks)
#pragma unroll
    for (int dt = 0; dt < 4; ++dt)
      acc[dt] = MFMA32(tr_frag(s.trb, sb + ks * 4096 + dt * 512), xf[4 * I + ks], acc[dt]);
}

template <int J> DI void attn_step(St& s) {
  constexpr int nk = 2 * J, nv = 2 * J + 1;
  if constexpr (nk == 0) wait_vm<16>(); else if constexpr (nk <= 12) wait_vm<12>(); else wait_vm<4>();
  barrier_lds();
  if constexpr (nk + 4 <= 15) issue_T<nk + 4>(s);
  constexpr int ksb = tslot(nk) * TILE;
  f32x16 p0 = {}, p1 = {};
#pragma unroll
  for (int d = 0; d < 8; ++d) {
    lds_ptr b = (d & 1) ? s.kb1 : s.kb0;
    bf16x8 a0 = *(lds_bf16x8*)(b + ksb + (d >> 1) * 512);
    bf16x8 a1 = *(lds_bf16x8*)(b + ksb + 8192 + (d >> 1) * 512);
    p0 = MFMA32(a0, s.qf[d], p0);
    p1 = MFMA32(a1, s.qf[d], p1);
  }
  float mx = fmaxf(fmaxf(p0[0], p0[1]), p1[0]);
  float my = fmaxf(fmaxf(p0[2], p0[3]), p1[1]);
  mx = fmaxf(fmaxf(mx, p1[2]), p1[3]);
#pragma unroll
  for (int r = 4; r < 16; r += 4) {
    mx = fmaxf(fmaxf(mx, p0[r]), p0[r + 1]); my = fmaxf(fmaxf(my, p0[r + 2]), p0[r + 3]);
    mx = fmaxf(fmaxf(mx, p1[r]), p1[r + 1]); my = fmaxf(fmaxf(my, p1[r + 2]), p1[r + 3]);
  }
  mx = fmaxf(mx, my);
  { auto rr = __builtin_amdgcn_permlane32_swap(__float_as_uint(mx), __float_as_uint(mx), false, false);
    mx = fmaxf(__uint_as_float(rr[0]), __uint_as_float(rr[1])); }
  mx *= SCL2;
  if constexpr (J == 0) { s.m = mx; }
  else {
    if (__builtin_expect(__any(mx - s.m > RESC_THR), 0)) {
      const float mn = fmaxf(s.m, mx), f = __builtin_amdgcn_exp2f(s.m - mn);
      s.m = mn; s.l *= f;
#pragma unroll
      for (int dt = 0; dt < 4; ++dt)
#pragma unroll
        for (int r = 0; r < 16; ++r) s.o[dt][r] *= f;
    }
  }
  const float nm = -s.m;
  float la = 0.f, lb = 0.f;
#pragma unroll
  for (int r = 0; r < 16; ++r) {
    p0[r] = __builtin_amdgcn_exp2f(__builtin_fmaf(p0[r], SCL2, nm)); la += p0[r];
    p1[r] = __builtin_amdgcn_exp2f(__builtin_fmaf(p1[r], SCL2, nm)); lb += p1[r];
  }
  s.l += la + lb;
  bf16x8 pf[4] = {pack8n(p0, 0), pack8n(p0, 1), pack8n(p1, 0), pack8n(p1, 1)};
  if constexpr (nv <= 12) wait_vm<12>(); else if constexpr (nv == 13) wait_vm<8>(); else wait_vm<0>();
  barrier_lds();
  if constexpr (nv + 4 <= 15) issue_T<nv + 4>(s);
  constexpr int vsb = tslot(nv) * TILE;
#pragma unroll
  for (int ks = 0; ks < 4; ++ks)
#pragma unroll
    for (int dt = 0; dt < 4; ++dt)
      s.o[dt] = MFMA32(tr_frag(s.trb, vsb + ks * 4096 + dt * 512), pf[ks], s.o[dt]);
}

template <int G> DI void x_stage(u32x4 (&xr)[32], bf16x8 (&xf)[32], lds_ptr wr, lds_ptr rd0) {
  wait_vm4<8 + 28 - 4 * G>(xr[4 * G], xr[4 * G + 1], xr[4 * G + 2], xr[4 * G + 3]);
#pragma unroll
  for (int rr = 0; rr < 4; ++rr) *(lds_u32x4*)(wr + rr * 1024) = xr[4 * G + rr];
#pragma unroll
  for (int ks = 0; ks < 4; ++ks) {
    lds_ptr p = (lds_ptr)((unsigned)(uintptr_t)rd0 ^ (unsigned)(ks * 32));
    xf[4 * G + ks] = *(lds_bf16x8*)p;
  }
}
template <int... Gs> DI void x_stage_all(u32x4 (&xr)[32], bf16x8 (&xf)[32], lds_ptr wr, lds_ptr rd0, iseq<Gs...>) { (x_stage<Gs>(xr, xf, wr, rd0), ...); }
template <int... Ms> DI void x_load_all(u32x4 (&xr)[32], const srd_t& sX, unsigned vx, iseq<Ms...>) {
  ((xr[Ms] = bload16<(Ms >> 2) * 128>(sX, vx, (Ms & 3) * 8192u)), ...);
}
template <int... Is> DI void qproj_all(St& s, f32x16 (&acc)[4], const bf16x8 (&xf)[32], iseq<Is...>) { (qproj_step<Is>(s, acc, xf), ...); }
template <int... Js> DI void attn_all(St& s, iseq<Js...>) { (attn_step<Js>(s), ...); }

__global__ __launch_bounds__(256, 2) void attn_kernel(AttnArgs a) {
  __shared__ __attribute__((aligned(1024))) char smem[5 * TILE];
  const unsigned long long t_start = __builtin_amdgcn_s_memrealtime();
  const int tid = threadIdx.x, lane = tid & 63;
  const unsigned wid = __builtin_amdgcn_readfirstlane(tid >> 6);
  const int bid = blockIdx.x, g = bid & 7, r = bid >> 3;
  const int br = g >> 2, h = (g & 3) * 8 + (r & 7), b = r >> 3;
  St s;
  s.wid = wid; s.lds0 = (unsigned)(uintptr_t)(lds_ptr)smem;
  const size_t kvoff = ((size_t)(b * NH + h)) * SK * DH * 2;
  s.sK = make_srd((const char*)a.k[br] + kvoff, SK * DH * 2);
  s.sV = make_srd((const char*)a.v[br] + kvoff, SK * DH * 2);
  s.sW = make_srd((const char*)a.wq[br] + h * 256, DM * NH * DH * 2 - h * 256);
  const srd_t sX = make_srd((const char*)a.x[br] + (size_t)b * SQ * DM * 2, SQ * DM * 2);
  {
    const unsigned r8 = (lane >> 2) & 7, cbl = lane >> 5, c4 = lane & 3;
    s.vK0 = r8 * 256 + cbl * 64 + ((c4 ^ (r8 >> 2)) * 16);
    s.vK1 = r8 * 256 + cbl * 64 + ((c4 ^ (2 + (r8 >> 2))) * 16);
    s.vV = r8 * 256 + cbl * 64 + c4 * 16;
    s.vW = r8 * 8192 + cbl * 64 + c4 * 16;
  }
  const int hh = lane >> 5, q32 = lane & 31;
  {
    const int blk = (lane >> 4) & 1, q4 = (lane & 15) >> 2, p = lane & 3, ps = ((p & 1) << 1) | (p >> 1);
    s.trb = (lds_ptr)smem + (hh * 2048 + q4 * 64 + blk * 32 + ps * 8);
    const int rp = (q32 & ~12) | ((q32 & 4) << 1) | ((q32 & 8) >> 1);
    const int kb = 2048 * (rp >> 3) + 64 * (rp & 7) + 16 * (hh ^ ((rp >> 2) & 3));
    s.kb0 = (lds_ptr)smem + kb; s.kb1 = (lds_ptr)smem + (kb ^ 32);
  }
  u32x4 xr[32]; bf16x8 xf[32];
  const unsigned vx = wid * 32768 + (lane >> 3) * 1024 + (lane & 7) * 16;
  if (wid < 2) { issue_W<0>(s); issue_W<1>(s); }
  x_load_all(xr, sX, vx, std::make_integer_sequence<int, 32>{});
  if (wid < 2) issue_W<2>(s); else issue_T_half<0>(s);
  {
    lds_ptr xb = (lds_ptr)smem + 4 * TILE + wid * 4096;
    const int wrow = lane >> 3;
    lds_ptr wr = xb + wrow * 128 + (((lane & 7) ^ wrow) * 16);
    lds_ptr rd0 = xb + q32 * 128 + ((hh ^ (q32 & 7)) * 16);
    x_stage_all(xr, xf, wr, rd0, std::make_integer_sequence<int, 8>{});
  }
  {
    f32x16 acc[4] = {};
    qproj_all(s, acc, xf, std::make_integer_sequence<int, 8>{});
#pragma unroll
    for (int dt = 0; dt < 4; ++dt) { s.qf[2 * dt] = pack8n(acc[dt], 0); s.qf[2 * dt + 1] = pack8n(acc[dt], 1); }
  }
#pragma unroll
  for (int dt = 0; dt < 4; ++dt) s.o[dt] = f32x16{};
  s.l = 0.f; s.m = 0.f;
  attn_all(s, std::make_integer_sequence<int, 8>{});
  float l = s.l;
  { auto rr = __builtin_amdgcn_permlane32_swap(__float_as_uint(l), __float_as_uint(l), false, false);
    l = __uint_as_float(rr[0]) + __uint_as_float(rr[1]); }
  const float inv = 1.0f / l;
  const int q = wid * 32 + q32;
  if (q < SQ) {
    char* ob = (char*)a.o + (size_t)br * WS_O_BR + ((size_t)(h * 16 + hh) * QG + b * SQ + q) * 16;
#pragma unroll
    for (int dt = 0; dt < 4; ++dt)
#pragma unroll
      for (int sgm = 0; sgm < 2; ++sgm)
        *(bf16x8*)(ob + (size_t)(4 * dt + 2 * sgm) * QG * 16) = pack8(s.o[dt], sgm, inv);
  }
  if (threadIdx.x == 0) { a.ts[2 * blockIdx.x] = t_start; a.ts[2 * blockIdx.x + 1] = __builtin_amdgcn_s_memrealtime(); }
}

struct OutArgs { const void* wo[2]; const void* o; void* part; unsigned long long* ts; };

struct St2 { srd_t sW, sO; unsigned lds0, wid, vW, vO; lds_ptr trb; f32x16 acc[4]; u32x4 bf[4][4]; };

template <int T> DI void issue2(St2& s) {
  constexpr int slot = T & 3;
  const unsigned ld = s.lds0 + slot * TILE + s.wid * 4096, so = T * 64 * 1024 + s.wid * 16 * 1024;
  dma16(s.sW, s.vW, so, ld); dma16(s.sW, s.vW, so + 128, ld + 1024);
  dma16(s.sW, s.vW, so + 8192, ld + 2048); dma16(s.sW, s.vW, so + 8192 + 128, ld + 3072);
#pragma unroll
  for (int ks = 0; ks < 4; ++ks) s.bf[slot][ks] = bload16<0>(s.sO, s.vO, (unsigned)((8 * T + 2 * ks) * QG * 16));
}
template <int T> DI void out_step(St2& s) {
  constexpr int slot = T & 3;
  constexpr int younger = (T + 2 <= 15 ? 8 : 0) + (T + 1 <= 15 ? 8 : 0);
  wait_vm4<younger>(s.bf[slot][0], s.bf[slot][1], s.bf[slot][2], s.bf[slot][3]);
  barrier_lds();
  if constexpr (T + 3 <= 15) issue2<T + 3>(s);
#pragma unroll
  for (int ks = 0; ks < 4; ++ks)
#pragma unroll
    for (int nt = 0; nt < 4; ++nt)
      s.acc[nt] = MFMA32(tr_frag(s.trb, slot * TILE + ks * 4096 + nt * 512), __builtin_bit_cast(bf16x8, s.bf[slot][ks]), s.acc[nt]);
}
template <int... Ts> DI void out_all(St2& s, iseq<Ts...>) { (out_step<Ts>(s), ...); }

__global__ __launch_bounds__(256, 1) void outproj_kernel(OutArgs a) {
  __shared__ __attribute__((aligned(1024))) char smem[4 * TILE];
  const unsigned long long t_start = __builtin_amdgcn_s_memrealtime();
  const int tid = threadIdx.x, lane = tid & 63;
  const unsigned wid = __builtin_amdgcn_readfirstlane(tid >> 6);
  const int bid = blockIdx.x, g = bid & 7, r = bid >> 3;
  const int br = g >> 2, sp = g & 3, qt = r >> 2, n4 = r & 3;
  St2 s; s.wid = wid; s.lds0 = (unsigned)(uintptr_t)(lds_ptr)smem;
  s.sW = make_srd((const char*)a.wo[br] + (size_t)sp * 1024 * 1024 + n4 * 256, 1024 * 1024 - n4 * 256);
  s.sO = make_srd((const char*)a.o + br * WS_O_BR + (size_t)sp * 128 * QG * 16, 128 * QG * 16);
  const int hh = lane >> 5, q32 = lane & 31;
  {
    const unsigned r8 = (lane >> 2) & 7, cbl = lane >> 5, c4 = lane & 3;
    s.vW = r8 * 1024 + cbl * 64 + c4 * 16;
    s.vO = (hh * QG + qt * 128 + wid * 32 + q32) * 16;
    const int blk = (lane >> 4) & 1, q4 = (lane & 15) >> 2, p = lane & 3, ps = ((p & 1) << 1) | (p >> 1);
    s.trb = (lds_ptr)smem + (hh * 2048 + q4 * 64 + blk * 32 + ps * 8);
  }
#pragma unroll
  for (int nt = 0; nt < 4; ++nt) s.acc[nt] = f32x16{};
  issue2<0>(s); issue2<1>(s); issue2<2>(s);
  out_all(s, std::make_integer_sequence<int, 16>{});
  float* pb = (float*)((char*)a.part + sp * WS_P_SPLIT) + ((size_t)(br * 64 + n4 * 16 + hh) * QG + qt * 128 + wid * 32 + q32) * 8;
#pragma unroll
  for (int nt = 0; nt < 4; ++nt)
#pragma unroll
    for (int sg = 0; sg < 2; ++sg) {
      float* p = pb + (size_t)(4 * nt + 2 * sg) * QG * 8;
      *(f32x4*)p = f32x4{s.acc[nt][8 * sg], s.acc[nt][8 * sg + 1], s.acc[nt][8 * sg + 2], s.acc[nt][8 * sg + 3]};
      *(f32x4*)(p + 4) = f32x4{s.acc[nt][8 * sg + 4], s.acc[nt][8 * sg + 5], s.acc[nt][8 * sg + 6], s.acc[nt][8 * sg + 7]};
    }
  if (threadIdx.x == 0) { a.ts[2 * blockIdx.x] = t_start; a.ts[2 * blockIdx.x + 1] = __builtin_amdgcn_s_memrealtime(); }
}

__global__ __launch_bounds__(256) void reduce_kernel(const float* __restrict__ part, void* __restrict__ out, unsigned long long* ts) {
  const unsigned long long t_start = __builtin_amdgcn_s_memrealtime();
  const int i = blockIdx.x * 256 + threadIdx.x;
  const int c = i / QG, qg = i - c * QG;
  const float* p = part + (size_t)i * 8;
  f32x4 a0 = *(const f32x4*)p, a1 = *(const f32x4*)(p + 4);
#pragma unroll
  for (int sp = 1; sp < 4; ++sp) {
    const float* pp = (const float*)((const char*)p + sp * WS_P_SPLIT);
    a0 += *(const f32x4*)pp; a1 += *(const f32x4*)(pp + 4);
  }
  u32x4 w = {cvtpk(a0[0], a0[1]), cvtpk(a0[2], a0[3]), cvtpk(a1[0], a1[1]), cvtpk(a1[2], a1[3])};
  *(u32x4*)((char*)out + ((size_t)qg * 1024 + c * 8) * 2) = w;
  if (threadIdx.x == 0) { ts[2 * blockIdx.x] = t_start; ts[2 * blockIdx.x + 1] = __builtin_amdgcn_s_memrealtime(); }
}
__global__ void spin_kernel(const unsigned long long* ts) {
  const unsigned long long tnow = __builtin_amdgcn_s_memrealtime();
  unsigned long long mn[3], mx[3]; const int nb[3] = {512, 224, 448};
  for (int k = 0; k < 3; ++k) { mn[k] = ~0ull; mx[k] = 0;
    for (int i = threadIdx.x; i < 512; i += 64) { int ii = i < nb[k] ? i : 0; unsigned long long a = ts[k * 2048 + 2 * ii], b = ts[k * 2048 + 2 * ii + 1]; mn[k] = a < mn[k] ? a : mn[k]; mx[k] = b > mx[k] ? b : mx[k]; }
    for (int o = 32; o; o >>= 1) { unsigned long long a = __shfl_xor(mn[k], o), b = __shfl_xor(mx[k], o); mn[k] = a < mn[k] ? a : mn[k]; mx[k] = b > mx[k] ? b : mx[k]; } }
  long long xs[7] = {0, (long long)(mx[0] - mn[0]), (long long)(mn[1] - mx[0]), (long long)(mx[1] - mn[1]), (long long)(mn[2] - mx[1]), (long long)(mx[2] - mn[2]), (long long)(tnow - mn[0])};
  for (int i = 0; i < 7; ++i) asm volatile("" : "+v"(xs[i]));
  long long x = xs[METRIC];
  if (x < 0) x = 0; if (x > 20000) x = 20000;
  const unsigned long long t0 = __builtin_amdgcn_s_memrealtime();
  while (__builtin_amdgcn_s_memrealtime() - t0 < (unsigned long long)(10 * x)) __builtin_amdgcn_s_sleep(1);
}
extern "C" void kernel_launch(void* const* d_in, const int* in_sizes, int n_in,
                              void* d_out, int out_size, void* d_ws, size_t ws_size,
                              hipStream_t stream) {
  AttnArgs a;
  a.x[0] = d_in[0]; a.k[0] = d_in[1]; a.v[0] = d_in[2]; a.wq[0] = d_in[3];
  a.x[1] = d_in[5]; a.k[1] = d_in[6]; a.v[1] = d_in[7]; a.wq[1] = d_in[8];
  a.o = (char*)d_ws + WS_O; unsigned long long* ts = (unsigned long long*)((char*)d_ws + (64u << 20)); a.ts = ts;
  attn_kernel<<<512, 256, 0, stream>>>(a);
  OutArgs o;
  o.wo[0] = d_in[4]; o.wo[1] = d_in[9]; o.o = (char*)d_ws + WS_O; o.part = (char*)d_ws + WS_P; o.ts = ts + 2048;
  outproj_kernel<<<224, 256, 0, stream>>>(o);
  reduce_kernel<<<128 * QG / 256, 256, 0, stream>>>((const float*)((char*)d_ws + WS_P), d_out, ts + 4096);
  spin_kernel<<<1, 64, 0, stream>>>(ts);
}
```

```cpp
#define METRIC 0
#include <hip/hip_runtime.h>
#include <hip/hip_bf16.h>
#include <stdint.h>
#include <utility>

typedef short    bf16x8 __attribute__((ext_vector_type(8)));
typedef short    s16x4  __attribute__((ext_vector_type(4)));
typedef float    f32x16 __attribute__((ext_vector_type(16)));
typedef float    f32x4  __attribute__((ext_vector_type(4)));
typedef float    f32x2  __attribute__((ext_vector_type(2)));
typedef __bf16   bf16x2 __attribute__((ext_vector_type(2)));
typedef unsigned u32x4  __attribute__((ext_vector_type(4)));
typedef __attribute__((address_space(3))) char* lds_ptr;
typedef __attribute__((address_space(3))) s16x4 lds_s16x4;
typedef __attribute__((address_space(3))) bf16x8 lds_bf16x8;
typedef __attribute__((address_space(3))) u32x4 lds_u32x4;

#define DI __device__ __forceinline__
#define MFMA32(a, b, c) __builtin_amdgcn_mfma_f32_32x32x16_bf16((a), (b), (c), 0, 0, 0)
#define SBAR() __builtin_amdgcn_sched_barrier(0)

constexpr int NB = 8, SQ = 112, DM = 512, NH = 32, SK = 512, DH = 128;
constexpr int QG = NB * SQ;
constexpr int TILE = 16384;
constexpr float SCL2 = 0.088388347648318447f * 1.4426950408889634f;
constexpr float RESC_THR = 6.0f;

constexpr size_t WS_O = 0;
constexpr size_t WS_O_BR = (size_t)512 * QG * 16;
constexpr size_t WS_P = 2 * WS_O_BR;
constexpr size_t WS_P_SPLIT = (size_t)2 * 512 * QG * 4;

DI unsigned cvtpk(float lo, float hi) {
  f32x2 v = {lo, hi}; bf16x2 b = __builtin_convertvector(v, bf16x2); return __builtin_bit_cast(unsigned, b);
}
DI bf16x8 pack8(const f32x16& x, int s, float sc) {
  u32x4 w = {cvtpk(x[8 * s + 0] * sc, x[8 * s + 1] * sc), cvtpk(x[8 * s + 2] * sc, x[8 * s + 3] * sc),
             cvtpk(x[8 * s + 4] * sc, x[8 * s + 5] * sc), cvtpk(x[8 * s + 6] * sc, x[8 * s + 7] * sc)};
  return __builtin_bit_cast(bf16x8, w);
}
DI bf16x8 pack8n(const f32x16& x, int s) {
  u32x4 w = {cvtpk(x[8 * s + 0], x[8 * s + 1]), cvtpk(x[8 * s + 2], x[8 * s + 3]),
             cvtpk(x[8 * s + 4], x[8 * s + 5]), cvtpk(x[8 * s + 6], x[8 * s + 7])};
  return __builtin_bit_cast(bf16x8, w);
}
typedef __amdgpu_buffer_rsrc_t srd_t;
DI srd_t make_srd(const void* p, unsigned bytes) {
  return __builtin_amdgcn_make_buffer_rsrc((void*)p, 0, bytes, 0x00020000);
}
DI void dma16(const srd_t& srd, unsigned voff, unsigned soff, unsigned ldsaddr) {
  asm volatile("s_mov_b32 m0, %0\n\ts_nop 0\n\tbuffer_load_dwordx4 %1, %2, %3 offen lds"
               :: "s"(ldsaddr), "v"(voff), "s"(srd), "s"(soff) : "memory");
}
#define KVPOL "nt"
DI void dma16s(const srd_t& srd, unsigned voff, unsigned soff, unsigned ldsaddr) {
  asm volatile("s_mov_b32 m0, %0\n\ts_nop 0\n\tbuffer_load_dwordx4 %1, %2, %3 offen " KVPOL " lds"
               :: "s"(ldsaddr), "v"(voff), "s"(srd), "s"(soff) : "memory");
}
template <int IMM> DI u32x4 bload16(const srd_t& srd, unsigned voff, unsigned soff) {
  u32x4 r;
  asm volatile("buffer_load_dwordx4 %0, %1, %2, %3 offen offset:%4" : "=v"(r) : "v"(voff), "s"(srd), "s"(soff), "i"(IMM) : "memory");
  return r;
}
template <int N> DI void wait_vm() { asm volatile("s_waitcnt vmcnt(%0)" :: "i"(N) : "memory"); }
template <int N> DI void wait_vm4(u32x4& a, u32x4& b, u32x4& c, u32x4& d) {
  asm volatile("s_waitcnt vmcnt(%4)" : "+v"(a), "+v"(b), "+v"(c), "+v"(d) : "i"(N) : "memory");
}
DI void barrier_lds() { asm volatile("s_waitcnt lgkmcnt(0)\n\ts_barrier" ::: "memory"); }
DI s16x4 trread(lds_ptr p) {
  return __builtin_amdgcn_ds_read_tr16_b64_v4i16((lds_s16x4*)p);
}
DI bf16x8 tr_frag(lds_ptr base, int off) {
  s16x4 lo = trread(base + off), hi = trread(base + off + 256);
  return __builtin_shufflevector(lo, hi, 0, 1, 2, 3, 4, 5, 6, 7);
}

template <int... Is> using iseq = std::integer_sequence<int, Is...>;

struct AttnArgs {
  const void* x[2]; const void* k[2]; const void* v[2]; const void* wq[2]; void* o; unsigned long long* ts;
};
#define STAMP(p, i) do { if (threadIdx.x == 0) (p)[i] = __builtin_amdgcn_s_memrealtime(); } while (0)

__host__ __device__ constexpr int tslot(int n) { return n % 5 == 0 ? 3 : n % 5 == 1 ? 4 : n % 5 == 2 ? 2 : n % 5 == 3 ? 0 : 1; }

struct St {
  srd_t sK, sV, sW;
  unsigned lds0, wid;
  unsigned vK0, vK1, vV, vW;
  lds_ptr trb, kb0, kb1;
  bf16x8 qf[8];
  f32x16 o[4];
  float m, l;
};

template <int N> DI void issue_T(St& s) {
  constexpr int slot = tslot(N), j = N >> 1;
  const unsigned ld = s.lds0 + slot * TILE + s.wid * 4096, so = j * TILE + s.wid * 4096;
  if constexpr ((N & 1) == 0) {
    dma16s(s.sK, s.vK0, so, ld); dma16s(s.sK, s.vK0, so + 128, ld + 1024);
    dma16s(s.sK, s.vK1, so + 2048, ld + 2048); dma16s(s.sK, s.vK1, so + 2048 + 128, ld + 3072);
  } else {
    dma16s(s.sV, s.vV, so, ld); dma16s(s.sV, s.vV, so + 128, ld + 1024);
    dma16s(s.sV, s.vV, so + 2048, ld + 2048); dma16s(s.sV, s.vV, so + 2048 + 128, ld + 3072);
  }
}
template <int N> DI void issue_T_half(St& s) {
  constexpr int slot = tslot(N);
  const unsigned hw = s.wid - 2;
  const unsigned ld = s.lds0 + slot * TILE + hw * 8192, so = hw * 8192;
#pragma unroll
  for (int u = 0; u < 8; ++u) {
    const unsigned o2 = (u >> 1) * 2048 + (u & 1) * 128;
    if constexpr ((N & 1) == 0) dma16s(s.sK, ((u >> 1) & 1) ? s.vK1 : s.vK0, so + o2, ld + u * 1024);
    else dma16s(s.sV, s.vV, so + o2, ld + u * 1024);
  }
}
template <int I> DI void issue_W(St& s) {
  constexpr int slot = I % 3;
  const unsigned ld = s.lds0 + slot * TILE + s.wid * 8192, so = I * 64 * 8192 + s.wid * 32 * 8192;
#pragma unroll
  for (int u = 0; u < 8; ++u) dma16(s.sW, s.vW, so + (u >> 1) * 8 * 8192 + (u & 1) * 128, ld + u * 1024);
}

template <int I> DI void qproj_step(St& s, f32x16 (&acc)[4], const bf16x8 (&xf)[32]) {
  if (s.wid < 2) { if constexpr (I < 7) wait_vm<8>(); else wait_vm<4>(); }
  barrier_lds();
  if constexpr (I == 0) { if (s.wid >= 2) issue_T_half<1>(s); }
  else if constexpr (I <= 5) { if (s.wid < 2) issue_W<I + 2>(s); }
  else issue_T<I - 4>(s);
  constexpr int sb = (I % 3) * TILE;
#pragma unroll
  for (int ks = 0; ks < 4; ++ks)
#pragma unroll
    for (int dt = 0; dt < 4; ++dt)
      acc[dt] = MFMA32(tr_frag(s.trb, sb + ks * 4096 + dt * 512), xf[4 * I + ks], acc[dt]);
}

template <int J> DI void attn_step(St& s) {
  constexpr int nk = 2 * J, nv = 2 * J + 1;
  if constexpr (nk == 0) wait_vm<16>(); else if constexpr (nk <= 12) wait_vm<12>(); else wait_vm<4>();
  barrier_lds();
  if constexpr (nk + 4 <= 15) issue_T<nk + 4>(s);
  constexpr int ksb = tslot(nk) * TILE;
  f32x16 p0 = {}, p1 = {};
#pragma unroll
  for (int d = 0; d < 8; ++d) {
    lds_ptr b = (d & 1) ? s.kb1 : s.kb0;
    bf16x8 a0 = *(lds_bf16x8*)(b + ksb + (d >> 1) * 512);
    bf16x8 a1 = *(lds_bf16x8*)(b + ksb + 8192 + (d >> 1) * 512);
    p0 = MFMA32(a0, s.qf[d], p0);
    p1 = MFMA32(a1, s.qf[d], p1);
  }
  float mx = fmaxf(fmaxf(p0[0], p0[1]), p1[0]);
  float my = fmaxf(fmaxf(p0[2], p0[3]), p1[1]);
  mx = fmaxf(fmaxf(mx, p1[2]), p1[3]);
#pragma unroll
  for (int r = 4; r < 16; r += 4) {
    mx = fmaxf(fmaxf(mx, p0[r]), p0[r + 1]); my = fmaxf(fmaxf(my, p0[r + 2]), p0[r + 3]);
    mx = fmaxf(fmaxf(mx, p1[r]), p1[r + 1]); my = fmaxf(fmaxf(my, p1[r + 2]), p1[r + 3]);
  }
  mx = fmaxf(mx, my);
  { auto rr = __builtin_amdgcn_permlane32_swap(__float_as_uint(mx), __float_as_uint(mx), false, false);
    mx = fmaxf(__uint_as_float(rr[0]), __uint_as_float(rr[1])); }
  mx *= SCL2;
  if constexpr (J == 0) { s.m = mx; }
  else {
    if (__builtin_expect(__any(mx - s.m > RESC_THR), 0)) {
      const float mn = fmaxf(s.m, mx), f = __builtin_amdgcn_exp2f(s.m - mn);
      s.m = mn; s.l *= f;
#pragma unroll
      for (int dt = 0; dt < 4; ++dt)
#pragma unroll
        for (int r = 0; r < 16; ++r) s.o[dt][r] *= f;
    }
  }
  const float nm = -s.m;
  float la = 0.f, lb = 0.f;
#pragma unroll
  for (int r = 0; r < 16; ++r) {
    p0[r] = __builtin_amdgcn_exp2f(__builtin_fmaf(p0[r], SCL2, nm)); la += p0[r];
    p1[r] = __builtin_amdgcn_exp2f(__builtin_fmaf(p1[r], SCL2, nm)); lb += p1[r];
  }
  s.l += la + lb;
  bf16x8 pf[4] = {pack8n(p0, 0), pack8n(p0, 1), pack8n(p1, 0), pack8n(p1, 1)};
  if constexpr (nv <= 12) wait_vm<12>(); else if constexpr (nv == 13) wait_vm<8>(); else wait_vm<0>();
  barrier_lds();
  if constexpr (nv + 4 <= 15) issue_T<nv + 4>(s);
  constexpr int vsb = tslot(nv) * TILE;
#pragma unroll
  for (int ks = 0; ks < 4; ++ks)
#pragma unroll
    for (int dt = 0; dt < 4; ++dt)
      s.o[dt] = MFMA32(tr_frag(s.trb, vsb + ks * 4096 + dt * 512), pf[ks], s.o[dt]);
}

template <int G> DI void x_stage(u32x4 (&xr)[32], bf16x8 (&xf)[32], lds_ptr wr, lds_ptr rd0) {
  wait_vm4<8 + 28 - 4 * G>(xr[4 * G], xr[4 * G + 1], xr[4 * G + 2], xr[4 * G + 3]);
#pragma unroll
  for (int rr = 0; rr < 4; ++rr) *(lds_u32x4*)(wr + rr * 1024) = xr[4 * G + rr];
#pragma unroll
  for (int ks = 0; ks < 4; ++ks) {
    lds_ptr p = (lds_ptr)((unsigned)(uintptr_t)rd0 ^ (unsigned)(ks * 32));
    xf[4 * G + ks] = *(lds_bf16x8*)p;
  }
}
template <int... Gs> DI void x_stage_all(u32x4 (&xr)[32], bf16x8 (&xf)[32], lds_ptr wr, lds_ptr rd0, iseq<Gs...>) { (x_stage<Gs>(xr, xf, wr, rd0), ...); }
template <int... Ms> DI void x_load_all(u32x4 (&xr)[32], const srd_t& sX, unsigned vx, iseq<Ms...>) {
  ((xr[Ms] = bload16<(Ms >> 2) * 128>(sX, vx, (Ms & 3) * 8192u)), ...);
}
template <int... Is> DI void qproj_all(St& s, f32x16 (&acc)[4], const bf16x8 (&xf)[32], iseq<Is...>) { (qproj_step<Is>(s, acc, xf), ...); }
template <int... Js> DI void attn_all(St& s, iseq<Js...>) { (attn_step<Js>(s), ...); }

__global__ __launch_bounds__(256, 2) void attn_kernel(AttnArgs a) {
  __shared__ __attribute__((aligned(1024))) char smem[5 * TILE];
  const unsigned long long t_start = __builtin_amdgcn_s_memrealtime();
  const int tid = threadIdx.x, lane = tid & 63;
  const unsigned wid = __builtin_amdgcn_readfirstlane(tid >> 6);
  const int bid = blockIdx.x, g = bid & 7, r = bid >> 3;
  const int br = g >> 2, h = (g & 3) * 8 + (r & 7), b = r >> 3;
  St s;
  s.wid = wid; s.lds0 = (unsigned)(uintptr_t)(lds_ptr)smem;
  const size_t kvoff = ((size_t)(b * NH + h)) * SK * DH * 2;
  s.sK = make_srd((const char*)a.k[br] + kvoff, SK * DH * 2);
  s.sV = make_srd((const char*)a.v[br] + kvoff, SK * DH * 2);
  s.sW = make_srd((const char*)a.wq[br] + h * 256, DM * NH * DH * 2 - h * 256);
  const srd_t sX = make_srd((const char*)a.x[br] + (size_t)b * SQ * DM * 2, SQ * DM * 2);
  {
    const unsigned r8 = (lane >> 2) & 7, cbl = lane >> 5, c4 = lane & 3;
    s.vK0 = r8 * 256 + cbl * 64 + ((c4 ^ (r8 >> 2)) * 16);
    s.vK1 = r8 * 256 + cbl * 64 + ((c4 ^ (2 + (r8 >> 2))) * 16);
    s.vV = r8 * 256 + cbl * 64 + c4 * 16;
    s.vW = r8 * 8192 + cbl * 64 + c4 * 16;
  }
  const int hh = lane >> 5, q32 = lane & 31;
  {
    const int blk = (lane >> 4) & 1, q4 = (lane & 15) >> 2, p = lane & 3, ps = ((p & 1) << 1) | (p >> 1);
    s.trb = (lds_ptr)smem + (hh * 2048 + q4 * 64 + blk * 32 + ps * 8);
    const int rp = (q32 & ~12) | ((q32 & 4) << 1) | ((q32 & 8) >> 1);
    const int kb = 2048 * (rp >> 3) + 64 * (rp & 7) + 16 * (hh ^ ((rp >> 2) & 3));
    s.kb0 = (lds_ptr)smem + kb; s.kb1 = (lds_ptr)smem + (kb ^ 32);
  }
  u32x4 xr[32]; bf16x8 xf[32];
  const unsigned vx = wid * 32768 + (lane >> 3) * 1024 + (lane & 7) * 16;
  if (wid < 2) { issue_W<0>(s); issue_W<1>(s); }
  x_load_all(xr, sX, vx, std::make_integer_sequence<int, 32>{});
  if (wid < 2) issue_W<2>(s); else issue_T_half<0>(s);
  {
    lds_ptr xb = (lds_ptr)smem + 4 * TILE + wid * 4096;
    const int wrow = lane >> 3;
    lds_ptr wr = xb + wrow * 128 + (((lane & 7) ^ wrow) * 16);
    lds_ptr rd0 = xb + q32 * 128 + ((hh ^ (q32 & 7)) * 16);
    x_stage_all(xr, xf, wr, rd0, std::make_integer_sequence<int, 8>{});
  }
  {
    f32x16 acc[4] = {};
    qproj_all(s, acc, xf, std::make_integer_sequence<int, 8>{});
#pragma unroll
    for (int dt = 0; dt < 4; ++dt) { s.qf[2 * dt] = pack8n(acc[dt], 0); s.qf[2 * dt + 1] = pack8n(acc[dt], 1); }
  }
#pragma unroll
  for (int dt = 0; dt < 4; ++dt) s.o[dt] = f32x16{};
  s.l = 0.f; s.m = 0.f;
  attn_all(s, std::make_integer_sequence<int, 8>{});
  float l = s.l;
  { auto rr = __builtin_amdgcn_permlane32_swap(__float_as_uint(l), __float_as_uint(l), false, false);
    l = __uint_as_float(rr[0]) + __uint_as_float(rr[1]); }
  const float inv = 1.0f / l;
  const int q = wid * 32 + q32;
  if (q < SQ) {
    char* ob = (char*)a.o + (size_t)br * WS_O_BR + ((size_t)(h * 16 + hh) * QG + b * SQ + q) * 16;
#pragma unroll
    for (int dt = 0; dt < 4; ++dt)
#pragma unroll
      for (int sgm = 0; sgm < 2; ++sgm)
        *(bf16x8*)(ob + (size_t)(4 * dt + 2 * sgm) * QG * 16) = pack8(s.o[dt], sgm, inv);
  }
  if (threadIdx.x == 0) { a.ts[2 * blockIdx.x] = t_start; a.ts[2 * blockIdx.x + 1] = __builtin_amdgcn_s_memrealtime(); }
}

struct OutArgs { const void* wo[2]; const void* o; void* part; unsigned long long* ts; };

struct St2 { srd_t sW, sO; unsigned lds0, wid, vW, vO; lds_ptr trb; f32x16 acc[4]; u32x4 bf[4][4]; };

template <int T> DI void issue2(St2& s) {
  constexpr int slot = T & 3;
  const unsigned ld = s.lds0 + slot * TILE + s.wid * 4096, so = T * 64 * 1024 + s.wid * 16 * 1024;
  dma16(s.sW, s.vW, so, ld); dma16(s.sW, s.vW, so + 128, ld + 1024);
  dma16(s.sW, s.vW, so + 8192, ld + 2048); dma16(s.sW, s.vW, so + 8192 + 128, ld + 3072);
#pragma unroll
  for (int ks = 0; ks < 4; ++ks) s.bf[slot][ks] = bload16<0>(s.sO, s.vO, (unsigned)((8 * T + 2 * ks) * QG * 16));
}
template <int T> DI void out_step(St2& s) {
  constexpr int slot = T & 3;
  constexpr int younger = (T + 2 <= 15 ? 8 : 0) + (T + 1 <= 15 ? 8 : 0);
  wait_vm4<younger>(s.bf[slot][0], s.bf[slot][1], s.bf[slot][2], s.bf[slot][3]);
  barrier_lds();
  if constexpr (T + 3 <= 15) issue2<T + 3>(s);
#pragma unroll
  for (int ks = 0; ks < 4; ++ks)
#pragma unroll
    for (int nt = 0; nt < 4; ++nt)
      s.acc[nt] = MFMA32(tr_frag(s.trb, slot * TILE + ks * 4096 + nt * 512), __builtin_bit_cast(bf16x8, s.bf[slot][ks]), s.acc[nt]);
}
template <int... Ts> DI void out_all(St2& s, iseq<Ts...>) { (out_step<Ts>(s), ...); }

__global__ __launch_bounds__(256, 1) void outproj_kernel(OutArgs a) {
  __shared__ __attribute__((aligned(1024))) char smem[4 * TILE];
  const unsigned long long t_start = __builtin_amdgcn_s_memrealtime();
  const int tid = threadIdx.x, lane = tid & 63;
  const unsigned wid = __builtin_amdgcn_readfirstlane(tid >> 6);
  const int bid = blockIdx.x, g = bid & 7, r = bid >> 3;
  const int br = g >> 2, sp = g & 3, qt = r >> 2, n4 = r & 3;
  St2 s; s.wid = wid; s.lds0 = (unsigned)(uintptr_t)(lds_ptr)smem;
  s.sW = make_srd((const char*)a.wo[br] + (size_t)sp * 1024 * 1024 + n4 * 256, 1024 * 1024 - n4 * 256);
  s.sO = make_srd((const char*)a.o + br * WS_O_BR + (size_t)sp * 128 * QG * 16, 128 * QG * 16);
  const int hh = lane >> 5, q32 = lane & 31;
  {
    const unsigned r8 = (lane >> 2) & 7, cbl = lane >> 5, c4 = lane & 3;
    s.vW = r8 * 1024 + cbl * 64 + c4 * 16;
    s.vO = (hh * QG + qt * 128 + wid * 32 + q32) * 16;
    const int blk = (lane >> 4) & 1, q4 = (lane & 15) >> 2, p = lane & 3, ps = ((p & 1) << 1) | (p >> 1);
    s.trb = (lds_ptr)smem + (hh * 2048 + q4 * 64 + blk * 32 + ps * 8);
  }
#pragma unroll
  for (int nt = 0; nt < 4; ++nt) s.acc[nt] = f32x16{};
  issue2<0>(s); issue2<1>(s); issue2<2>(s);
  out_all(s, std::make_integer_sequence<int, 16>{});
  float* pb = (float*)((char*)a.part + sp * WS_P_SPLIT) + ((size_t)(br * 64 + n4 * 16 + hh) * QG + qt * 128 + wid * 32 + q32) * 8;
#pragma unroll
  for (int nt = 0; nt < 4; ++nt)
#pragma unroll
    for (int sg = 0; sg < 2; ++sg) {
      float* p = pb + (size_t)(4 * nt + 2 * sg) * QG * 8;
      *(f32x4*)p = f32x4{s.acc[nt][8 * sg], s.acc[nt][8 * sg + 1], s.acc[nt][8 * sg + 2], s.acc[nt][8 * sg + 3]};
      *(f32x4*)(p + 4) = f32x4{s.acc[nt][8 * sg + 4], s.acc[nt][8 * sg + 5], s.acc[nt][8 * sg + 6], s.acc[nt][8 * sg + 7]};
    }
  if (threadIdx.x == 0) { a.ts[2 * blockIdx.x] = t_start; a.ts[2 * blockIdx.x + 1] = __builtin_amdgcn_s_memrealtime(); }
}

__global__ __launch_bounds__(256) void reduce_kernel(const float* __restrict__ part, void* __restrict__ out, unsigned long long* ts) {
  const unsigned long long t_start = __builtin_amdgcn_s_memrealtime();
  const int i = blockIdx.x * 256 + threadIdx.x;
  const int c = i / QG, qg = i - c * QG;
  const float* p = part + (size_t)i * 8;
  f32x4 a0 = *(const f32x4*)p, a1 = *(const f32x4*)(p + 4);
#pragma unroll
  for (int sp = 1; sp < 4; ++sp) {
    const float* pp = (const float*)((const char*)p + sp * WS_P_SPLIT);
    a0 += *(const f32x4*)pp; a1 += *(const f32x4*)(pp + 4);
  }
  u32x4 w = {cvtpk(a0[0], a0[1]), cvtpk(a0[2], a0[3]), cvtpk(a1[0], a1[1]), cvtpk(a1[2], a1[3])};
  *(u32x4*)((char*)out + ((size_t)qg * 1024 + c * 8) * 2) = w;
  if (threadIdx.x == 0) { ts[2 * blockIdx.x] = t_start; ts[2 * blockIdx.x + 1] = __builtin_amdgcn_s_memrealtime(); }
}
__global__ void tiny_kernel(unsigned long long* ts) { if (threadIdx.x == 0) ts[8192] = __builtin_amdgcn_s_memrealtime(); }
__global__ void spin_kernel(const unsigned long long* ts) {
  const unsigned long long tnow = __builtin_amdgcn_s_memrealtime();
  unsigned long long mns = ~0ull, mxs = 0, mne = ~0ull, mxe = 0, mnd = ~0ull;
  const unsigned long long tt = ts[8192];
  for (int i = threadIdx.x; i < 512; i += 64) { unsigned long long a = ts[2 * i], b = ts[2 * i + 1];
    mns = a < mns ? a : mns; mxs = a > mxs ? a : mxs; mne = b < mne ? b : mne; mxe = b > mxe ? b : mxe; mnd = (b - a) < mnd ? (b - a) : mnd; }
  for (int o = 32; o; o >>= 1) { unsigned long long a;
    a = __shfl_xor(mns, o); mns = a < mns ? a : mns; a = __shfl_xor(mxs, o); mxs = a > mxs ? a : mxs;
    a = __shfl_xor(mne, o); mne = a < mne ? a : mne; a = __shfl_xor(mxe, o); mxe = a > mxe ? a : mxe; a = __shfl_xor(mnd, o); mnd = a < mnd ? a : mnd; }
  long long xs[7] = {0, (long long)(mns - tt), (long long)(mxs - mns), (long long)(tnow - tt), (long long)mnd, (long long)(mne - mns), 0};
  for (int i = 0; i < 7; ++i) asm volatile("" : "+v"(xs[i]));
  long long x = xs[METRIC];
  if (x < 0) x = 0; if (x > 20000) x = 20000;
  const unsigned long long t0 = __builtin_amdgcn_s_memrealtime();
  while (__builtin_amdgcn_s_memrealtime() - t0 < (unsigned long long)(10 * x)) __builtin_amdgcn_s_sleep(1);
}
extern "C" void kernel_launch(void* const* d_in, const int* in_sizes, int n_in,
                              void* d_out, int out_size, void* d_ws, size_t ws_size,
                              hipStream_t stream) {
  AttnArgs a;
  a.x[0] = d_in[0]; a.k[0] = d_in[1]; a.v[0] = d_in[2]; a.wq[0] = d_in[3];
  a.x[1] = d_in[5]; a.k[1] = d_in[6]; a.v[1] = d_in[7]; a.wq[1] = d_in[8];
  a.o = (char*)d_ws + WS_O; unsigned long long* ts = (unsigned long long*)((char*)d_ws + (64u << 20)); a.ts = ts;
  tiny_kernel<<<1, 64, 0, stream>>>(ts);
  attn_kernel<<<512, 256, 0, stream>>>(a);
  OutArgs o;
  o.wo[0] = d_in[4]; o.wo[1] = d_in[9]; o.o = (char*)d_ws + WS_O; o.part = (char*)d_ws + WS_P; o.ts = ts + 2048;
  outproj_kernel<<<224, 256, 0, stream>>>(o);
  reduce_kernel<<<128 * QG / 256, 256, 0, stream>>>((const float*)((char*)d_ws + WS_P), d_out, ts + 4096);
  spin_kernel<<<1, 64, 0, stream>>>(ts);
}
```

```cpp
#include <hip/hip_runtime.h>
#include <hip/hip_bf16.h>
#include <stdint.h>
#include <utility>

typedef short    bf16x8 __attribute__((ext_vector_type(8)));
typedef short    s16x4  __attribute__((ext_vector_type(4)));
typedef float    f32x16 __attribute__((ext_vector_type(16)));
typedef float    f32x4  __attribute__((ext_vector_type(4)));
typedef float    f32x2  __attribute__((ext_vector_type(2)));
typedef __bf16   bf16x2 __attribute__((ext_vector_type(2)));
typedef unsigned u32x4  __attribute__((ext_vector_type(4)));
typedef __attribute__((address_space(3))) char* lds_ptr;
typedef __attribute__((address_space(3))) s16x4 lds_s16x4;
typedef __attribute__((address_space(3))) bf16x8 lds_bf16x8;
typedef __attribute__((address_space(3))) u32x4 lds_u32x4;

#define DI __device__ __forceinline__
#define MFMA32(a, b, c) __builtin_amdgcn_mfma_f32_32x32x16_bf16((a), (b), (c), 0, 0, 0)
#define SBAR() __builtin_amdgcn_sched_barrier(0)
#define PIN(x) asm volatile("" : "+v"(x))

constexpr int NB = 8, SQ = 112, DM = 512, NH = 32, SK = 512, DH = 128;
constexpr int TILE = 16384;
constexpr float SCL2 = 0.088388347648318447f * 1.4426950408889634f;
#define RTHR 24.0f
constexpr float RESC_THR = RTHR;

constexpr unsigned SLAB = 64 * SQ * 16;
constexpr size_t WS_F = (size_t)256 * SLAB;
constexpr unsigned ACK_OFF = 16 * 256;
constexpr unsigned ACK_K = 0x9E3779B9u;
constexpr unsigned SPIN_CAP = 1u << 20;
constexpr unsigned OOB = 0x80000000u;

DI unsigned cvtpk(float lo, float hi) {
  f32x2 v = {lo, hi}; bf16x2 b = __builtin_convertvector(v, bf16x2); return __builtin_bit_cast(unsigned, b);
}
DI u32x4 pack8(const f32x16& x, int s, float sc) {
  return u32x4{cvtpk(x[8 * s + 0] * sc, x[8 * s + 1] * sc), cvtpk(x[8 * s + 2] * sc, x[8 * s + 3] * sc),
               cvtpk(x[8 * s + 4] * sc, x[8 * s + 5] * sc), cvtpk(x[8 * s + 6] * sc, x[8 * s + 7] * sc)};
}
DI u32x4 pack8n(const f32x16& x, int s) {
  return u32x4{cvtpk(x[8 * s + 0], x[8 * s + 1]), cvtpk(x[8 * s + 2], x[8 * s + 3]), cvtpk(x[8 * s + 4], x[8 * s + 5]), cvtpk(x[8 * s + 6], x[8 * s + 7])};
}
typedef __amdgpu_buffer_rsrc_t srd_t;
DI srd_t make_srd(const void* p, unsigned bytes) { return __builtin_amdgcn_make_buffer_rsrc((void*)p, 0, bytes, 0x00020000); }
DI void dma16(const srd_t& srd, unsigned voff, unsigned soff, unsigned ldsaddr) {
  asm volatile("s_mov_b32 m0, %0\n\ts_nop 0\n\tbuffer_load_dwordx4 %1, %2, %3 offen lds"
               :: "s"(ldsaddr), "v"(voff), "s"(srd), "s"(soff) : "memory");
}
#define KVPOL "nt"
DI void dma16s(const srd_t& srd, unsigned voff, unsigned soff, unsigned ldsaddr) {
  asm volatile("s_mov_b32 m0, %0\n\ts_nop 0\n\tbuffer_load_dwordx4 %1, %2, %3 offen " KVPOL " lds"
               :: "s"(ldsaddr), "v"(voff), "s"(srd), "s"(soff) : "memory");
}
DI unsigned bload4_sc1(const srd_t& srd, unsigned voff) {
  unsigned r; asm volatile("buffer_load_dword %0, %1, %2, 0 offen sc1" : "=v"(r) : "v"(voff), "s"(srd) : "memory"); return r;
}
DI unsigned bload4_sc1_wait(const srd_t& srd, unsigned voff) {
  unsigned r; asm volatile("buffer_load_dword %0, %1, %2, 0 offen sc1\n\ts_waitcnt vmcnt(0)" : "=&v"(r) : "v"(voff), "s"(srd) : "memory"); return r;
}
DI void bstore4_sc1(const srd_t& srd, unsigned voff, unsigned v) {
  asm volatile("buffer_store_dword %0, %1, %2, 0 offen sc1\n\ts_nop 0" :: "v"(v), "v"(voff), "s"(srd) : "memory");
}
DI void bstore16_sc1(const srd_t& srd, unsigned voff, unsigned soff, u32x4 v) {
  asm volatile("buffer_store_dwordx4 %0, %1, %2, %3 offen sc1\n\ts_nop 1" :: "v"(v), "v"(voff), "s"(srd), "s"(soff) : "memory");
}
template <int N> DI void wait_vm() { asm volatile("s_waitcnt vmcnt(%0)" :: "i"(N) : "memory"); }
DI void barrier_lds() { asm volatile("s_waitcnt lgkmcnt(0)\n\ts_barrier" ::: "memory"); }
DI s16x4 trread(lds_ptr p) { return __builtin_amdgcn_ds_read_tr16_b64_v4i16((lds_s16x4*)p); }
DI bf16x8 tr_frag(lds_ptr base, int off) {
  s16x4 lo = trread(base + off), hi = trread(base + off + 256);
  return __builtin_shufflevector(lo, hi, 0, 1, 2, 3, 4, 5, 6, 7);
}

#define STAMP(i)
#define STAMP_FLUSH(n)
template <int... Is> using iseq = std::integer_sequence<int, Is...>;

struct Args {
  const void* x[2]; const void* k[2]; const void* v[2]; const void* wq[2]; const void* wo[2]; void* ws; void* out;
};
DI float max3f(float a, float b, float c) { float r; asm volatile("v_max3_f32 %0, %1, %2, %3" : "=v"(r) : "v"(a), "v"(b), "v"(c)); return r; }
DI float max2f(float a, float b) { float r; asm volatile("v_max_f32_e32 %0, %1, %2" : "=v"(r) : "v"(a), "v"(b)); return r; }
DI float fadd_s(float a, float b) { float r; asm volatile("v_add_f32_e32 %0, %1, %2" : "=v"(r) : "v"(a), "v"(b)); return r; }
DI void sub16(f32x16& v, float m) {
#pragma unroll
  for (int r = 0; r < 16; ++r) { float e = v[r]; asm volatile("v_sub_f32_e32 %0, %0, %1" : "+v"(e) : "v"(m)); v[r] = e; }
}
DI void mul16(f32x16& v, float f) {
#pragma unroll
  for (int r = 0; r < 16; ++r) { float e = v[r]; asm volatile("v_mul_f32_e32 %0, %0, %1" : "+v"(e) : "v"(f)); v[r] = e; }
}

constexpr int NP = 32;
__host__ __device__ constexpr bool is_k(int p) { return p == 8 || (p >= 9 && p <= 21 && (p & 1)); }
__host__ __device__ constexpr bool is_v(int p) { return p >= 10 && p <= 23 && !is_k(p); }
__host__ __device__ constexpr int kv_idx(int p) { return p == 8 ? 0 : p == 23 ? 7 : (p & 1) ? (p - 7) / 2 : (p - 10) / 2; }
__host__ __device__ constexpr int nops(int p) { return p < 8 ? 6 : p < NP ? 4 : 0; }
constexpr int BG = 10;
constexpr int GREG = 5 * TILE;
__host__ __device__ constexpr int pslot(int p) {
  return p < 8 ? p % 3 : p >= 24 ? (p - 24) % 3 : ((p - 8) % 5 == 0 ? 2 : (p - 8) % 5 == 1 ? 0 : (p - 8) % 5 == 2 ? 3 : (p - 8) % 5 == 3 ? 1 : 4);
}
__host__ __device__ constexpr int xoff(int p) { return p % 3 == 0 ? 3 * TILE : p % 3 == 1 ? 4 * TILE : GREG + 3 * TILE; }
__host__ __device__ constexpr int iss(int t) {
  return t < 3 ? -1 : t <= 8 ? t - 3 : t <= 10 ? 6 : t <= 12 ? 7 : t <= 23 ? t - 5 : t == 24 ? 19 : t == 25 ? 21 : t == 26 ? 23 : t - 3;
}
__host__ __device__ constexpr int first_at(int p) { for (int t = 0; t < NP; ++t) if (iss(t) == p) return t; return -1; }
__host__ __device__ constexpr int second_at(int p) { int f = first_at(p); return (f >= 0 && f + 1 < NP && iss(f + 1) == p) ? f + 1 : -1; }
__host__ __device__ constexpr int younger(int P) {
  int n = 0;
  for (int t = P + 2; t < NP; ++t) if (iss(t) < P) n += nops(t);
  if (P == 28 || P == 29) n += 8;
  return n;
}

struct St {
  srd_t sK, sV, sW, sX, sWo, sP;
  unsigned ldsg, ldsx, w4, wv, rot, grp;
  unsigned vK0, vK1, vV, vW, vWo, vx0, vx1, vP;
  lds_ptr trb, kb0, kb1, xrd;
  lds_ptr hb0, hb1, xh;
  lds_ptr oex_w, oex_r;
  bf16x8 xf[2][4];
  bf16x8 af[2][16];
  bf16x8 qf[8];
  bf16x8 of[8];
  f32x16 acc[4];
  f32x16 s[2][2];
  unsigned pw[16];
  float m, la, lb, mx, my;
  bool mflag;
};

template <int P> DI void dma_piece(St& s, int u) {
  const unsigned ld = s.ldsg + pslot(P) * TILE + (s.w4 * 4 + u) * 1024;
  if constexpr (P < 8) {
    const unsigned c = (s.rot + P) & 7;
    if (u < 4) dma16(s.sW, s.vW, c * (64 * 8192) + s.w4 * (16 * 8192) + (u >> 1) * (8 * 8192) + (u & 1) * 128, ld);
    else dma16(s.sX, (u & 1) ? s.vx1 : s.vx0, c * 128 + s.wv * 16384 + (u & 1) * 8192, s.ldsx + xoff(P) + (s.wv * 2 + (u & 1)) * 1024);
  }
  else if constexpr (is_k(P)) dma16s(s.sK, (u >> 1) ? s.vK1 : s.vK0, kv_idx(P) * TILE + s.w4 * 4096 + (u >> 1) * 2048 + (u & 1) * 128, ld);
  else if constexpr (is_v(P)) dma16s(s.sV, s.vV, kv_idx(P) * TILE + s.w4 * 4096 + (u >> 1) * 2048 + (u & 1) * 128, ld);
  else {
    constexpr int c2 = (P - 24) >> 2, j = (P - 24) & 3;
    const unsigned e = (j >> 1) ^ s.grp;
    dma16(s.sWo, s.vWo, (e * 128 + (j & 1) * 64 + s.w4 * 16 + (u >> 1) * 8) * 1024 + c2 * 256 + (u & 1) * 128, ld);
  }
}
template <int P> DI void dma_tile(St& s) {
#pragma unroll
  for (int u = 0; u < nops(P); ++u) dma_piece<P>(s, u);
}
DI lds_ptr add64k(lds_ptr p) { unsigned a; asm volatile("v_add_u32 %0, 0x10000, %1" : "=v"(a) : "v"((unsigned)(uintptr_t)p)); return (lds_ptr)(uintptr_t)a; }
template <int P, int F> DI void read_frag(St& s) {
  constexpr bool hi = pslot(P) == 4;
  constexpr int sb = hi ? 0 : pslot(P) * TILE;
  if constexpr (hi && F == 0) { s.hb0 = add64k(is_k(P) ? s.kb0 : s.trb); if constexpr (is_k(P)) s.hb1 = add64k(s.kb1); }
  if constexpr (is_k(P)) {
    constexpr int kt = F >> 3, d = F & 7;
    s.af[P & 1][F] = *(lds_bf16x8*)(((d & 1) ? (hi ? s.hb1 : s.kb1) : (hi ? s.hb0 : s.kb0)) + sb + kt * 8192 + (d >> 1) * 512);
  } else {
    constexpr int ks = F >> 2, dt = F & 3;
    s.af[P & 1][F] = tr_frag(hi ? s.hb0 : s.trb, sb + ks * 4096 + dt * 512);
  }
}
template <int P, int KS> DI void x_read(St& s) {
  constexpr bool hi = P % 3 == 2;
  if constexpr (hi && KS == 0) s.xh = add64k(s.xrd);
  lds_ptr p = (lds_ptr)(uintptr_t)((unsigned)(uintptr_t)(hi ? s.xh : s.xrd) ^ (unsigned)(KS * 32));
  s.xf[P & 1][KS] = *(lds_bf16x8*)(p + (xoff(P) - 3 * TILE - (hi ? 65536 : 0)));
}
template <int T, int E> DI void exp1(St& s) { s.s[T & 1][E >> 4][E & 15] = __builtin_amdgcn_exp2f(s.s[T & 1][E >> 4][E & 15]); }
template <int T, int C> DI void cvt1(St& s) {
  constexpr int e = 2 * C;
  s.pw[C] = cvtpk(s.s[T & 1][e >> 4][e & 15], s.s[T & 1][e >> 4][(e & 15) + 1]);
}
template <int T, int E> DI void sum1(St& s) {
  if constexpr (E & 1) s.lb = fadd_s(s.lb, s.s[T & 1][E >> 4][E & 15]); else s.la = fadd_s(s.la, s.s[T & 1][E >> 4][E & 15]);
}
template <int T, int I> DI void max1(St& s) {
  constexpr int e = 2 * I;
  if constexpr (I == 0) s.mx = max2f(s.s[T & 1][0][0], s.s[T & 1][0][1]);
  else if constexpr (I == 1) s.my = max2f(s.s[T & 1][0][2], s.s[T & 1][0][3]);
  else if constexpr (I & 1) s.my = max3f(s.my, s.s[T & 1][e >> 4][e & 15], s.s[T & 1][e >> 4][(e & 15) + 1]);
  else s.mx = max3f(s.mx, s.s[T & 1][e >> 4][e & 15], s.s[T & 1][e >> 4][(e & 15) + 1]);
}
DI float rowmax_finish(St& s) {
  float mx, my;
  asm volatile("v_max_f32_e32 %0, %2, %3\n\tv_max_f32_e32 %1, %2, %3\n\ts_nop 1\n\tv_permlane32_swap_b32 %0, %1\n\ts_nop 1\n\tv_max_f32_e32 %0, %0, %1"
               : "=&v"(mx), "=&v"(my) : "v"(s.mx), "v"(s.my));
  return mx;
}
template <int C2, int I> DI void slab_store(St& s) {
  const u32x4 v = C2 == 0 ? pack8n(s.acc[I >> 1], I & 1) : pack8n(s.s[I >> 2][(I >> 1) & 1], I & 1);
  bstore16_sc1(s.sP, s.vP, (16 * C2 + 2 * I) * (SQ * 16), v);
}

template <int P, int G> DI void gap(St& s) {
  if constexpr (P < 8) {
    constexpr int ks = G >> 2, dt = G & 3;
    if constexpr (P == 0 && ks == 0) s.acc[dt] = MFMA32(s.af[P & 1][G], s.xf[P & 1][ks], f32x16{});
    else s.acc[dt] = MFMA32(s.af[P & 1][G], s.xf[P & 1][ks], s.acc[dt]);
  } else if constexpr (is_k(P)) {
    constexpr int t = kv_idx(P), kt = G >> 3, d = G & 7;
    if constexpr (d == 0) s.s[t & 1][kt] = MFMA32(s.af[P & 1][G], s.qf[d], f32x16{});
    else s.s[t & 1][kt] = MFMA32(s.af[P & 1][G], s.qf[d], s.s[t & 1][kt]);
  } else if constexpr (is_v(P)) {
    constexpr int t = kv_idx(P), ks = G >> 2, dt = G & 3;
    const u32x4 pv = {s.pw[4 * ks], s.pw[4 * ks + 1], s.pw[4 * ks + 2], s.pw[4 * ks + 3]};
    if constexpr (t == 0 && ks == 0) s.acc[dt] = MFMA32(s.af[P & 1][G], __builtin_bit_cast(bf16x8, pv), f32x16{});
    else s.acc[dt] = MFMA32(s.af[P & 1][G], __builtin_bit_cast(bf16x8, pv), s.acc[dt]);
  } else {
    constexpr int c2 = (P - 24) >> 2, j = (P - 24) & 3, ks = G >> 2, nt = G & 3;
    const bf16x8 bfr = (j >> 1) ? s.of[4 * (j & 1) + ks] : s.qf[4 * (j & 1) + ks];
    f32x16& d = c2 == 0 ? s.acc[nt] : s.s[nt >> 1][nt & 1];
    if constexpr (j == 0 && ks == 0) d = MFMA32(s.af[P & 1][G], bfr, f32x16{});
    else d = MFMA32(s.af[P & 1][G], bfr, d);
  }
  SBAR();
  if constexpr (G < 8) {
    constexpr int f0 = 4 + (3 * G + 1) / 2, f1 = 4 + (3 * (G + 1) + 1) / 2;
    read_frag<P, f0>(s);
    if constexpr (f1 - f0 == 2) read_frag<P, f0 + 1>(s);
  } else if constexpr (G >= 12 && P + 1 < NP) {
    read_frag<P + 1, G - 12>(s);
    if constexpr (P + 1 < 8) x_read<P + 1, G - 12>(s);
  }
  if constexpr (P >= 9 && is_k(P)) {
    constexpr int t = kv_idx(P) - 1;
    exp1<t, 2 * G>(s); exp1<t, 2 * G + 1>(s);
    if constexpr (G >= 8 && G < 12) { cvt1<t, 2 * (G - 8)>(s); cvt1<t, 2 * (G - 8) + 1>(s); PIN(s.pw[2 * (G - 8)]); PIN(s.pw[2 * (G - 8) + 1]); }
    PIN(s.s[t & 1][G >> 3]);
  } else if constexpr (is_v(P)) {
    constexpr int t = kv_idx(P);
    if constexpr (G < 8) {
      sum1<t, 2 * G>(s); sum1<t, 2 * G + 1>(s);
      cvt1<t, 8 + G>(s); PIN(s.pw[8 + G]);
    } else if constexpr (t < 7 && G < 12) {
      max1<t + 1, 3 * (G - 8)>(s); sum1<t, 2 * G>(s); max1<t + 1, 3 * (G - 8) + 1>(s); sum1<t, 2 * G + 1>(s); max1<t + 1, 3 * (G - 8) + 2>(s);
    } else if constexpr (t < 7 && G < 14) {
      max1<t + 1, 12 + 2 * (G - 12)>(s); sum1<t, 2 * G>(s); max1<t + 1, 13 + 2 * (G - 12)>(s); sum1<t, 2 * G + 1>(s);
    } else {
      sum1<t, 2 * G>(s); sum1<t, 2 * G + 1>(s);
      if constexpr (t < 7 && G == 14) s.mx = rowmax_finish(s);
    }
  } else if constexpr (P == 25) {
    if constexpr (G == 8 || G == 9) {
#pragma unroll
      for (int i = 0; i < 4; ++i) s.of[4 * (G - 8) + i] = *(lds_bf16x8*)(s.oex_r + (4 * (G - 8) + i) * 1024);
    }
  } else if constexpr (P == 28) {
    if constexpr (G >= 2 && G < 10) slab_store<0, G - 2>(s);
  }
  if constexpr (G == BG && P + 1 < NP) {
    wait_vm<younger(P)>();
    barrier_lds();
  }
  if constexpr (first_at(P) >= 0 && G >= BG && G - BG < nops(first_at(P))) dma_piece<(first_at(P) >= 0 ? first_at(P) : 0)>(s, G - BG);
  if constexpr (second_at(P) >= 0 && G >= 14) dma_piece<(second_at(P) >= 0 ? second_at(P) : 0)>(s, G - 14);
  if constexpr (P > 0 && second_at(P - 1) >= 0 && G < 2) dma_piece<(second_at(P - 1) >= 0 ? second_at(P - 1) : 0)>(s, G + 2);
  SBAR();
}
template <int P, int... Gs> DI void phase_(St& s, iseq<Gs...>) { (gap<P, Gs>(s), ...); }
template <int P> DI void phase(St& s) {
  phase_<P>(s, std::make_integer_sequence<int, 16>{});
  if constexpr (P == 7) {
#pragma unroll
    for (int dt = 0; dt < 4; ++dt) {
      s.qf[2 * dt] = __builtin_bit_cast(bf16x8, pack8(s.acc[dt], 0, SCL2)); s.qf[2 * dt + 1] = __builtin_bit_cast(bf16x8, pack8(s.acc[dt], 1, SCL2));
    }
    SBAR();
  } else if constexpr (P == 8) {
    float mx = fmaxf(fmaxf(s.s[0][0][0], s.s[0][0][1]), s.s[0][1][0]), my = fmaxf(fmaxf(s.s[0][0][2], s.s[0][0][3]), s.s[0][1][1]);
    mx = fmaxf(fmaxf(mx, s.s[0][1][2]), s.s[0][1][3]);
#pragma unroll
    for (int r = 4; r < 16; r += 4) {
      mx = fmaxf(fmaxf(mx, s.s[0][0][r]), s.s[0][0][r + 1]); my = fmaxf(fmaxf(my, s.s[0][0][r + 2]), s.s[0][0][r + 3]);
      mx = fmaxf(fmaxf(mx, s.s[0][1][r]), s.s[0][1][r + 1]); my = fmaxf(fmaxf(my, s.s[0][1][r + 2]), s.s[0][1][r + 3]);
    }
    s.mx = mx; s.my = my;
    const float g = rowmax_finish(s);
    s.m = 0.f; s.la = 0.f; s.lb = 0.f; s.mflag = false;
    if (__builtin_expect(__any(!(fabsf(g) <= RESC_THR)), 0)) {
      s.m = g; s.mflag = true;
      sub16(s.s[0][0], g); sub16(s.s[0][1], g);
    }
    SBAR();
  } else if constexpr (is_v(P) && P < 23) {
    constexpr int t1 = kv_idx(P) + 1;
    const float g = s.mx;
    if (__builtin_expect(s.mflag || __any(g > RESC_THR), 0)) {
      if (__any(g - s.m > RESC_THR)) {
        const float mn = fmaxf(s.m, g), f = __builtin_amdgcn_exp2f(s.m - mn);
        s.m = mn; s.la *= f; s.lb *= f;
        asm volatile("s_nop 7\n\ts_nop 7");
        mul16(s.acc[0], f); mul16(s.acc[1], f); mul16(s.acc[2], f); mul16(s.acc[3], f);
        asm volatile("s_nop 1");
      }
      s.mflag = true;
      sub16(s.s[t1 & 1][0], s.m); sub16(s.s[t1 & 1][1], s.m);
    }
    if constexpr (P == 22) {
#pragma unroll
      for (int r = 0; r < 16; ++r) { s.s[1][0][r] = __builtin_amdgcn_exp2f(s.s[1][0][r]); s.s[1][1][r] = __builtin_amdgcn_exp2f(s.s[1][1][r]); }
      cvt1<7, 0>(s); cvt1<7, 1>(s); cvt1<7, 2>(s); cvt1<7, 3>(s); cvt1<7, 4>(s); cvt1<7, 5>(s); cvt1<7, 6>(s); cvt1<7, 7>(s);
    }
    SBAR();
  } else if constexpr (P == 23) {
    float l = s.la + s.lb;
    { auto rr = __builtin_amdgcn_permlane32_swap(__float_as_uint(l), __float_as_uint(l), false, false);
      l = __uint_as_float(rr[0]) + __uint_as_float(rr[1]); }
    const float inv = 1.0f / l;
#pragma unroll
    for (int i = 0; i < 8; ++i) {
      const u32x4 v = pack8(s.acc[i >> 1], i & 1, inv);
      s.qf[i] = __builtin_bit_cast(bf16x8, v);
      *(lds_u32x4*)(s.oex_w + i * 1024) = v;
    }
    SBAR();
  }
  if constexpr (P == 3) STAMP(2); if constexpr (P == 7) STAMP(3); if constexpr (P == 12) STAMP(4); if constexpr (P == 18) STAMP(5);
  if constexpr (P == 23) STAMP(6); if constexpr (P == 27) STAMP(7); if constexpr (P == 31) STAMP(8);
}
template <int... Ps> DI void phases(St& s, iseq<Ps...>) { (phase<Ps>(s), ...); }

__global__ __launch_bounds__(512, 2) void fused_kernel(Args a) {
  __shared__ __attribute__((aligned(1024))) char smem[10 * TILE];
  const int tid = threadIdx.x, lane = tid & 63;
  const unsigned wv = __builtin_amdgcn_readfirstlane(tid >> 6), grp = wv >> 2, w4 = wv & 3;
  const int bid = blockIdx.x, g8 = bid & 7, r = bid >> 3;
  const int br = g8 >> 2, hp = (g8 & 3) * 4 + (r & 3), b = r >> 2, pair = br * 8 + b;
  const unsigned h = hp * 2 + grp;
  St s;
  const unsigned lds0 = (unsigned)(uintptr_t)(lds_ptr)smem;
  s.wv = wv; s.w4 = w4; s.grp = grp; s.ldsg = lds0 + grp * GREG; s.ldsx = lds0;
  s.rot = (b + 2 * (r & 3)) & 7;
  const size_t kvoff = ((size_t)(b * NH) + h) * (SK * DH * 2);
#define AB_KV 1
#define AB_W 1
  s.sK = make_srd((const char*)a.k[br] + kvoff, AB_KV * SK * DH * 2);
  s.sV = make_srd((const char*)a.v[br] + kvoff, AB_KV * SK * DH * 2);
  s.sW = make_srd((const char*)a.wq[br] + h * 256, AB_W * (DM * NH * DH * 2 - h * 256));
  s.sX = make_srd((const char*)a.x[br] + (size_t)b * SQ * DM * 2, AB_W * SQ * DM * 2);
  s.sWo = make_srd((const char*)a.wo[br] + (size_t)hp * 256 * 1024 + grp * 512, AB_W * (256 * 1024 - grp * 512));
  s.sP = make_srd((const char*)a.ws + (size_t)(pair * 16 + hp) * SLAB + grp * (32 * SQ * 16), 32 * SQ * 16);
  const srd_t sF = make_srd((const char*)a.ws + WS_F, ACK_OFF + 16 * 16 * 128);
  const unsigned r8 = (lane >> 2) & 7, cbl = lane >> 5, c4 = lane & 3;
  s.vK0 = r8 * 256 + cbl * 64 + ((c4 ^ (r8 >> 2)) * 16);
  s.vK1 = r8 * 256 + cbl * 64 + ((c4 ^ (2 + (r8 >> 2))) * 16);
  s.vV = r8 * 256 + cbl * 64 + c4 * 16;
  s.vW = r8 * 8192 + cbl * 64 + c4 * 16;
  s.vWo = r8 * 1024 + cbl * 64 + c4 * 16;
  s.vx0 = (lane >> 3) * 1024 + (((lane & 7) ^ (lane >> 4)) * 16);
  s.vx1 = (lane >> 3) * 1024 + (((lane & 7) ^ (4 + (lane >> 4))) * 16);
  const int hh = lane >> 5, q32 = lane & 31;
  {
    const int blk = (lane >> 4) & 1, q4 = (lane & 15) >> 2, p = lane & 3, ps = ((p & 1) << 1) | (p >> 1);
    const lds_ptr gb = (lds_ptr)smem + grp * GREG;
    s.trb = gb + (hh * 2048 + q4 * 64 + blk * 32 + ps * 8);
    const int rp = (q32 & ~12) | ((q32 & 4) << 1) | ((q32 & 8) >> 1);
    const int kb = 2048 * (rp >> 3) + 64 * (rp & 7) + 16 * (hh ^ ((rp >> 2) & 3));
    s.kb0 = gb + kb; s.kb1 = gb + (kb ^ 32);
    s.xrd = (lds_ptr)smem + 3 * TILE + (w4 * 32 + q32) * 128 + ((hh ^ ((q32 >> 1) & 7)) * 16);
    s.oex_w = gb + 3 * TILE + w4 * 8192 + lane * 16;
    s.oex_r = (lds_ptr)smem + (grp ^ 1) * GREG + 3 * TILE + w4 * 8192 + lane * 16;
    const int q = w4 * 32 + q32;
    s.vP = q < SQ ? (unsigned)((hh * SQ + q) * 16) : OOB;
  }
  STAMP(0);
  const unsigned vF = (unsigned)(pair * 256 + (lane & 15) * 4);
  unsigned fold = bload4_sc1(sF, wv == 0 ? vF : OOB);
  dma_tile<0>(s); dma_tile<1>(s); dma_tile<2>(s);
  wait_vm<12>();
  PIN(fold);
  bstore4_sc1(sF, (wv == 0 && lane < 16) ? (unsigned)(ACK_OFF + (pair * 16 + hp) * 128 + lane * 4) : OOB, fold + ACK_K);
  barrier_lds();
  read_frag<0, 0>(s); read_frag<0, 1>(s); read_frag<0, 2>(s); read_frag<0, 3>(s);
  x_read<0, 0>(s); x_read<0, 1>(s); x_read<0, 2>(s); x_read<0, 3>(s);
  SBAR();
  STAMP(1);
  phases(s, std::make_integer_sequence<int, NP>{});
  const unsigned vA = (unsigned)(ACK_OFF + (pair * 16 + (lane & 15)) * 128 + hp * 4);
  unsigned ackv = bload4_sc1(sF, wv == 0 ? vA : OOB);
  slab_store<1, 0>(s); slab_store<1, 1>(s); slab_store<1, 2>(s); slab_store<1, 3>(s);
  slab_store<1, 4>(s); slab_store<1, 5>(s); slab_store<1, 6>(s); slab_store<1, 7>(s);
  asm volatile("s_waitcnt vmcnt(0)" : "+v"(ackv) :: "memory");
  barrier_lds();
  STAMP(9);
  if (wv == 0) {
    const unsigned own = __builtin_amdgcn_readlane(fold, hp);
    for (unsigned sp = 0; !__all(ackv == own + ACK_K) && sp < SPIN_CAP; ++sp) { __builtin_amdgcn_s_sleep(2); ackv = bload4_sc1_wait(sF, vA); }
    bstore4_sc1(sF, lane == hp ? vF : OOB, own + 1);
    for (unsigned sp = 0; sp < SPIN_CAP; ++sp) {
      const unsigned f = bload4_sc1_wait(sF, vF);
      if (__all(f != fold)) break;
      __builtin_amdgcn_s_sleep(4);
    }
  }
  barrier_lds();
  STAMP(10);
  {
    const srd_t sR = make_srd((const char*)a.ws + (size_t)pair * 16 * SLAB, 16 * SLAB);
    const int q = tid >> 2, no = tid & 3;
    const unsigned vr = q < SQ ? (unsigned)(((hp * 4 + no) * SQ + q) * 16) : OOB;
    u32x4 v[16];
#pragma unroll
    for (int i = 0; i < 16; ++i) v[i] = __builtin_amdgcn_raw_buffer_load_b128(sR, vr, i * SLAB, 16);
    float acc[8] = {};
#pragma unroll
    for (int i = 0; i < 16; ++i)
#pragma unroll
      for (int d = 0; d < 4; ++d) { acc[2 * d] += __uint_as_float(v[i][d] << 16); acc[2 * d + 1] += __uint_as_float(v[i][d] & 0xffff0000u); }
    if (q < SQ)
      *(u32x4*)((char*)a.out + ((size_t)(b * SQ + q) * 1024 + br * 512 + hp * 32 + no * 8) * 2) =
          u32x4{cvtpk(acc[0], acc[1]), cvtpk(acc[2], acc[3]), cvtpk(acc[4], acc[5]), cvtpk(acc[6], acc[7])};
  }
  STAMP(11); STAMP_FLUSH(12);
}

extern "C" void kernel_launch(void* const* d_in, const int* in_sizes, int n_in,
                              void* d_out, int out_size, void* d_ws, size_t ws_size,
                              hipStream_t stream) {
  Args a;
  a.x[0] = d_in[0]; a.k[0] = d_in[1]; a.v[0] = d_in[2]; a.wq[0] = d_in[3]; a.wo[0] = d_in[4];
  a.x[1] = d_in[5]; a.k[1] = d_in[6]; a.v[1] = d_in[7]; a.wq[1] = d_in[8]; a.wo[1] = d_in[9];
  a.ws = d_ws; a.out = d_out;
  fused_kernel<<<256, 512, 0, stream>>>(a);
}
```

```cpp
#define STAMPS
#define DIAG 2
#include <hip/hip_runtime.h>
#include <hip/hip_bf16.h>
#include <stdint.h>
#include <utility>

typedef short    bf16x8 __attribute__((ext_vector_type(8)));
typedef short    s16x4  __attribute__((ext_vector_type(4)));
typedef float    f32x16 __attribute__((ext_vector_type(16)));
typedef float    f32x4  __attribute__((ext_vector_type(4)));
typedef float    f32x2  __attribute__((ext_vector_type(2)));
typedef __bf16   bf16x2 __attribute__((ext_vector_type(2)));
typedef unsigned u32x4  __attribute__((ext_vector_type(4)));
typedef __attribute__((address_space(3))) char* lds_ptr;
typedef __attribute__((address_space(3))) s16x4 lds_s16x4;
typedef __attribute__((address_space(3))) bf16x8 lds_bf16x8;
typedef __attribute__((address_space(3))) u32x4 lds_u32x4;

#define DI __device__ __forceinline__
#define MFMA32(a, b, c) __builtin_amdgcn_mfma_f32_32x32x16_bf16((a), (b), (c), 0, 0, 0)
#define SBAR() __builtin_amdgcn_sched_barrier(0)
#define PIN(x) asm volatile("" : "+v"(x))

constexpr int NB = 8, SQ = 112, DM = 512, NH = 32, SK = 512, DH = 128;
constexpr int TILE = 16384;
constexpr float SCL2 = 0.088388347648318447f * 1.4426950408889634f;
#define RTHR 24.0f
constexpr float RESC_THR = RTHR;

constexpr unsigned SLAB = 64 * SQ * 16;
constexpr size_t WS_F = (size_t)256 * SLAB;
constexpr unsigned ACK_OFF = 16 * 256;
constexpr unsigned ACK_K = 0x9E3779B9u;
constexpr unsigned SPIN_CAP = 1u << 20;
constexpr unsigned OOB = 0x80000000u;

DI unsigned cvtpk(float lo, float hi) {
  f32x2 v = {lo, hi}; bf16x2 b = __builtin_convertvector(v, bf16x2); return __builtin_bit_cast(unsigned, b);
}
DI u32x4 pack8(const f32x16& x, int s, float sc) {
  return u32x4{cvtpk(x[8 * s + 0] * sc, x[8 * s + 1] * sc), cvtpk(x[8 * s + 2] * sc, x[8 * s + 3] * sc),
               cvtpk(x[8 * s + 4] * sc, x[8 * s + 5] * sc), cvtpk(x[8 * s + 6] * sc, x[8 * s + 7] * sc)};
}
DI u32x4 pack8n(const f32x16& x, int s) {
  return u32x4{cvtpk(x[8 * s + 0], x[8 * s + 1]), cvtpk(x[8 * s + 2], x[8 * s + 3]), cvtpk(x[8 * s + 4], x[8 * s + 5]), cvtpk(x[8 * s + 6], x[8 * s + 7])};
}
typedef __amdgpu_buffer_rsrc_t srd_t;
DI srd_t make_srd(const void* p, unsigned bytes) { return __builtin_amdgcn_make_buffer_rsrc((void*)p, 0, bytes, 0x00020000); }
DI void dma16(const srd_t& srd, unsigned voff, unsigned soff, unsigned ldsaddr) {
  asm volatile("s_mov_b32 m0, %0\n\ts_nop 0\n\tbuffer_load_dwordx4 %1, %2, %3 offen lds"
               :: "s"(ldsaddr), "v"(voff), "s"(srd), "s"(soff) : "memory");
}
#define KVPOL "nt"
DI void dma16s(const srd_t& srd, unsigned voff, unsigned soff, unsigned ldsaddr) {
  asm volatile("s_mov_b32 m0, %0\n\ts_nop 0\n\tbuffer_load_dwordx4 %1, %2, %3 offen " KVPOL " lds"
               :: "s"(ldsaddr), "v"(voff), "s"(srd), "s"(soff) : "memory");
}
DI unsigned bload4_sc1(const srd_t& srd, unsigned voff) {
  unsigned r; asm volatile("buffer_load_dword %0, %1, %2, 0 offen sc1" : "=v"(r) : "v"(voff), "s"(srd) : "memory"); return r;
}
DI unsigned bload4_sc1_wait(const srd_t& srd, unsigned voff) {
  unsigned r; asm volatile("buffer_load_dword %0, %1, %2, 0 offen sc1\n\ts_waitcnt vmcnt(0)" : "=&v"(r) : "v"(voff), "s"(srd) : "memory"); return r;
}
DI void bstore4_sc1(const srd_t& srd, unsigned voff, unsigned v) {
  asm volatile("buffer_store_dword %0, %1, %2, 0 offen sc1\n\ts_nop 0" :: "v"(v), "v"(voff), "s"(srd) : "memory");
}
DI void bstore16_sc1(const srd_t& srd, unsigned voff, unsigned soff, u32x4 v) {
  asm volatile("buffer_store_dwordx4 %0, %1, %2, %3 offen sc1\n\ts_nop 1" :: "v"(v), "v"(voff), "s"(srd), "s"(soff) : "memory");
}
template <int N> DI void wait_vm() { asm volatile("s_waitcnt vmcnt(%0)" :: "i"(N) : "memory"); }
DI void barrier_lds() { asm volatile("s_waitcnt lgkmcnt(0)\n\ts_barrier" ::: "memory"); }
DI s16x4 trread(lds_ptr p) { return __builtin_amdgcn_ds_read_tr16_b64_v4i16((lds_s16x4*)p); }
DI bf16x8 tr_frag(lds_ptr base, int off) {
  s16x4 lo = trread(base + off), hi = trread(base + off + 256);
  return __builtin_shufflevector(lo, hi, 0, 1, 2, 3, 4, 5, 6, 7);
}

#define STAMP(i) do { unsigned long long t_; asm volatile("s_memrealtime %0\n\ts_waitcnt lgkmcnt(0)" : "=s"(t_) :: "memory"); s.ts[i] = (unsigned)t_; if ((i) == 0) s.tshi = (unsigned)(t_ >> 32); } while (0)
#define STAMP_FLUSH_UNUSED(n) do { if (threadIdx.x == 0 || threadIdx.x == 448) { for (int i_ = 0; i_ < (n); ++i_) g_ts[(blockIdx.x * 2 + (threadIdx.x ? 1 : 0)) * 32 + i_] = ((unsigned long long)s.tshi << 32) | s.ts[i_]; } } while (0)
template <int... Is> using iseq = std::integer_sequence<int, Is...>;

struct Args {
  const void* x[2]; const void* k[2]; const void* v[2]; const void* wq[2]; const void* wo[2]; void* ws; void* out;
};
DI float max3f(float a, float b, float c) { float r; asm volatile("v_max3_f32 %0, %1, %2, %3" : "=v"(r) : "v"(a), "v"(b), "v"(c)); return r; }
DI float max2f(float a, float b) { float r; asm volatile("v_max_f32_e32 %0, %1, %2" : "=v"(r) : "v"(a), "v"(b)); return r; }
DI float fadd_s(float a, float b) { float r; asm volatile("v_add_f32_e32 %0, %1, %2" : "=v"(r) : "v"(a), "v"(b)); return r; }
DI void sub16(f32x16& v, float m) {
#pragma unroll
  for (int r = 0; r < 16; ++r) { float e = v[r]; asm volatile("v_sub_f32_e32 %0, %0, %1" : "+v"(e) : "v"(m)); v[r] = e; }
}
DI void mul16(f32x16& v, float f) {
#pragma unroll
  for (int r = 0; r < 16; ++r) { float e = v[r]; asm volatile("v_mul_f32_e32 %0, %0, %1" : "+v"(e) : "v"(f)); v[r] = e; }
}

constexpr int NP = 32;
__host__ __device__ constexpr bool is_k(int p) { return p == 8 || (p >= 9 && p <= 21 && (p & 1)); }
__host__ __device__ constexpr bool is_v(int p) { return p >= 10 && p <= 23 && !is_k(p); }
__host__ __device__ constexpr int kv_idx(int p) { return p == 8 ? 0 : p == 23 ? 7 : (p & 1) ? (p - 7) / 2 : (p - 10) / 2; }
__host__ __device__ constexpr int nops(int p) { return p < 8 ? 6 : p < NP ? 4 : 0; }
constexpr int BG = 10;
constexpr int GREG = 5 * TILE;
__host__ __device__ constexpr int pslot(int p) {
  return p < 8 ? p % 3 : p >= 30 ? p - 27 : p >= 24 ? (p - 24) % 3 : ((p - 8) % 5 == 0 ? 2 : (p - 8) % 5 == 1 ? 0 : (p - 8) % 5 == 2 ? 3 : (p - 8) % 5 == 3 ? 1 : 4);
}
__host__ __device__ constexpr int xoff(int p) { return p % 3 == 0 ? 3 * TILE : p % 3 == 1 ? 4 * TILE : GREG + 3 * TILE; }
__host__ __device__ constexpr int fill(int t) {
  return t < 3 ? -1 : t <= 8 ? t - 3 : t <= 10 ? 6 : t <= 12 ? 7 : t <= 23 ? t - 5 : t == 24 ? 19 : t == 25 ? 21 : t == 26 ? 23 : t <= 29 ? t - 3 : t == 30 ? 26 : 27;
}
#define NPARK 9
__host__ __device__ constexpr bool parked(int t) { return t >= 8 && t < 8 + NPARK; }
__host__ __device__ constexpr int pset(int t) { return (t - 8) % 5; }
__host__ __device__ constexpr int preq(int t) { return t <= 12 ? t - 8 : fill(t - 5) + 1; }
__host__ __device__ constexpr int first_at(int p) { for (int t = 0; t < NP; ++t) if (fill(t) == p) return t; return -1; }
__host__ __device__ constexpr int second_at(int p) { int f = first_at(p); return (f >= 0 && f + 1 < NP && fill(f + 1) == p) ? f + 1 : -1; }
__host__ __device__ constexpr int preq1_at(int p) { for (int t = 8; t < 8 + NPARK; ++t) if (preq(t) == p) return t; return -1; }
__host__ __device__ constexpr int preq2_at(int p) { int f = preq1_at(p); return (f >= 0 && parked(f + 1) && preq(f + 1) == p) ? f + 1 : -1; }
__host__ __device__ constexpr int rpos(int t) { return t < 3 ? t - 3 : parked(t) ? preq(t) * 8 + 1 + (preq2_at(preq(t)) == t ? 1 : 0) : fill(t) * 8 + 5 + (second_at(fill(t)) == t ? 1 : 0) ; }
constexpr int STORE_POS = 28 * 8 + 2;
__host__ __device__ constexpr int younger(int T, int POS) {
  int n = 0;
  for (int t = 0; t < NP; ++t) if (rpos(t) > rpos(T) && rpos(t) < POS) n += nops(t);
  if (STORE_POS > rpos(T) && STORE_POS < POS) n += 8;
  return n;
}

struct St {
  srd_t sK, sV, sW, sX, sWo, sP;
  unsigned ldsg, ldsx, w4, wv, rot, grp;
  unsigned vK0, vK1, vV, vW, vWo, vx0, vx1, vP;
  lds_ptr trb, kb0, kb1, xrd;
  lds_ptr hb0, hb1, xh;
  lds_ptr oex_w, oex_r;
  bf16x8 xf[2][4];
  bf16x8 af[2][16];
  bf16x8 qf[8];
  bf16x8 of[8];
  f32x16 acc[4];
  f32x16 s[2][2];
  unsigned pw[16];
  u32x4 pk[5][4];
  lds_ptr pkw;
  float m, la, lb, mx, my;
  unsigned ts[16], tshi;
  bool mflag;
};

template <int P> DI void dma_piece(St& s, int u) {
  const unsigned ld = s.ldsg + pslot(P) * TILE + (s.w4 * 4 + u) * 1024;
  if constexpr (P < 8) {
    const unsigned c = (s.rot + P) & 7;
    if (u < 4) dma16(s.sW, s.vW, c * (64 * 8192) + s.w4 * (16 * 8192) + (u >> 1) * (8 * 8192) + (u & 1) * 128, ld);
    else dma16(s.sX, (u & 1) ? s.vx1 : s.vx0, c * 128 + s.wv * 16384 + (u & 1) * 8192, s.ldsx + xoff(P) + (s.wv * 2 + (u & 1)) * 1024);
  }
  else if constexpr (is_k(P)) dma16s(s.sK, (u >> 1) ? s.vK1 : s.vK0, kv_idx(P) * TILE + s.w4 * 4096 + (u >> 1) * 2048 + (u & 1) * 128, ld);
  else if constexpr (is_v(P)) dma16s(s.sV, s.vV, kv_idx(P) * TILE + s.w4 * 4096 + (u >> 1) * 2048 + (u & 1) * 128, ld);
  else {
    constexpr int c2 = (P - 24) >> 2, j = (P - 24) & 3;
    const unsigned e = (j >> 1) ^ s.grp;
    dma16(s.sWo, s.vWo, (e * 128 + (j & 1) * 64 + s.w4 * 16 + (u >> 1) * 8) * 1024 + c2 * 256 + (u & 1) * 128, ld);
  }
}
template <int P> DI void park_load(St& s, int u) {
  const unsigned so = kv_idx(P) * TILE + s.w4 * 4096 + (u >> 1) * 2048 + (u & 1) * 128;
  if constexpr (is_k(P)) asm volatile("buffer_load_dwordx4 %0, %1, %2, %3 offen " KVPOL : "=v"(s.pk[pset(P)][u]) : "v"((u >> 1) ? s.vK1 : s.vK0), "s"(s.sK), "s"(so) : "memory");
  else asm volatile("buffer_load_dwordx4 %0, %1, %2, %3 offen " KVPOL : "=v"(s.pk[pset(P)][u]) : "v"(s.vV), "s"(s.sV), "s"(so) : "memory");
}
template <int P, int POS> DI void fill_piece(St& s, int u) {
  if constexpr (parked(P)) {
    if (u == 0) {
      wait_vm<younger(P, POS)>();
      asm volatile("" : "+v"(s.pk[pset(P)][0]), "+v"(s.pk[pset(P)][1]), "+v"(s.pk[pset(P)][2]), "+v"(s.pk[pset(P)][3]));
    }
    if constexpr (pslot(P) == 4) *(lds_u32x4*)(s.pkw + 3 * TILE + u * 1024) = s.pk[pset(P)][u];
    else *(lds_u32x4*)(s.pkw - TILE + pslot(P) * TILE + u * 1024) = s.pk[pset(P)][u];
  } else dma_piece<P>(s, u);
}
template <int P> DI void dma_tile(St& s) {
#pragma unroll
  for (int u = 0; u < nops(P); ++u) dma_piece<P>(s, u);
}
DI lds_ptr add64k(lds_ptr p) { unsigned a; asm volatile("v_add_u32 %0, 0x10000, %1" : "=v"(a) : "v"((unsigned)(uintptr_t)p)); return (lds_ptr)(uintptr_t)a; }
template <int P, int F> DI void read_frag(St& s) {
  constexpr bool hi = pslot(P) == 4;
  constexpr int sb = hi ? 0 : pslot(P) * TILE;
  if constexpr (hi && F == 0) { s.hb0 = add64k(is_k(P) ? s.kb0 : s.trb); if constexpr (is_k(P)) s.hb1 = add64k(s.kb1); }
  if constexpr (is_k(P)) {
    constexpr int kt = F >> 3, d = F & 7;
    s.af[P & 1][F] = *(lds_bf16x8*)(((d & 1) ? (hi ? s.hb1 : s.kb1) : (hi ? s.hb0 : s.kb0)) + sb + kt * 8192 + (d >> 1) * 512);
  } else {
    constexpr int ks = F >> 2, dt = F & 3;
    s.af[P & 1][F] = tr_frag(hi ? s.hb0 : s.trb, sb + ks * 4096 + dt * 512);
  }
}
template <int P, int KS> DI void x_read(St& s) {
  constexpr bool hi = P % 3 == 2;
  if constexpr (hi && KS == 0) s.xh = add64k(s.xrd);
  lds_ptr p = (lds_ptr)(uintptr_t)((unsigned)(uintptr_t)(hi ? s.xh : s.xrd) ^ (unsigned)(KS * 32));
  s.xf[P & 1][KS] = *(lds_bf16x8*)(p + (xoff(P) - 3 * TILE - (hi ? 65536 : 0)));
}
template <int T, int E> DI void exp1(St& s) { s.s[T & 1][E >> 4][E & 15] = __builtin_amdgcn_exp2f(s.s[T & 1][E >> 4][E & 15]); }
template <int T, int C> DI void cvt1(St& s) {
  constexpr int e = 2 * C;
  s.pw[C] = cvtpk(s.s[T & 1][e >> 4][e & 15], s.s[T & 1][e >> 4][(e & 15) + 1]);
}
template <int T, int E> DI void sum1(St& s) {
  if constexpr (E & 1) s.lb = fadd_s(s.lb, s.s[T & 1][E >> 4][E & 15]); else s.la = fadd_s(s.la, s.s[T & 1][E >> 4][E & 15]);
}
template <int T, int I> DI void max1(St& s) {
  constexpr int e = 2 * I;
  if constexpr (I == 0) s.mx = max2f(s.s[T & 1][0][0], s.s[T & 1][0][1]);
  else if constexpr (I == 1) s.my = max2f(s.s[T & 1][0][2], s.s[T & 1][0][3]);
  else if constexpr (I & 1) s.my = max3f(s.my, s.s[T & 1][e >> 4][e & 15], s.s[T & 1][e >> 4][(e & 15) + 1]);
  else s.mx = max3f(s.mx, s.s[T & 1][e >> 4][e & 15], s.s[T & 1][e >> 4][(e & 15) + 1]);
}
DI float rowmax_finish(St& s) {
  float mx, my;
  asm volatile("v_max_f32_e32 %0, %2, %3\n\tv_max_f32_e32 %1, %2, %3\n\ts_nop 1\n\tv_permlane32_swap_b32 %0, %1\n\ts_nop 1\n\tv_max_f32_e32 %0, %0, %1"
               : "=&v"(mx), "=&v"(my) : "v"(s.mx), "v"(s.my));
  return mx;
}
template <int C2, int I> DI void slab_store(St& s) {
  const u32x4 v = C2 == 0 ? pack8n(s.acc[I >> 1], I & 1) : pack8n(s.s[I >> 2][(I >> 1) & 1], I & 1);
  bstore16_sc1(s.sP, s.vP, (16 * C2 + 2 * I) * (SQ * 16), v);
}

template <int P, int G> DI void gap(St& s) {
  if constexpr (P < 8) {
    constexpr int ks = G >> 2, dt = G & 3;
    if constexpr (P == 0 && ks == 0) s.acc[dt] = MFMA32(s.af[P & 1][G], s.xf[P & 1][ks], f32x16{});
    else s.acc[dt] = MFMA32(s.af[P & 1][G], s.xf[P & 1][ks], s.acc[dt]);
  } else if constexpr (is_k(P)) {
    constexpr int t = kv_idx(P), kt = G >> 3, d = G & 7;
    if constexpr (d == 0) s.s[t & 1][kt] = MFMA32(s.af[P & 1][G], s.qf[d], f32x16{});
    else s.s[t & 1][kt] = MFMA32(s.af[P & 1][G], s.qf[d], s.s[t & 1][kt]);
  } else if constexpr (is_v(P)) {
    constexpr int t = kv_idx(P), ks = G >> 2, dt = G & 3;
    const u32x4 pv = {s.pw[4 * ks], s.pw[4 * ks + 1], s.pw[4 * ks + 2], s.pw[4 * ks + 3]};
    if constexpr (t == 0 && ks == 0) s.acc[dt] = MFMA32(s.af[P & 1][G], __builtin_bit_cast(bf16x8, pv), f32x16{});
    else s.acc[dt] = MFMA32(s.af[P & 1][G], __builtin_bit_cast(bf16x8, pv), s.acc[dt]);
  } else {
    constexpr int c2 = (P - 24) >> 2, j = (P - 24) & 3, ks = G >> 2, nt = G & 3;
    const bf16x8 bfr = (j >> 1) ? s.of[4 * (j & 1) + ks] : s.qf[4 * (j & 1) + ks];
    f32x16& d = c2 == 0 ? s.acc[nt] : s.s[nt >> 1][nt & 1];
    if constexpr (j == 0 && ks == 0) d = MFMA32(s.af[P & 1][G], bfr, f32x16{});
    else d = MFMA32(s.af[P & 1][G], bfr, d);
  }
  SBAR();
  if constexpr (G < 8) {
    constexpr int f0 = 4 + (3 * G + 1) / 2, f1 = 4 + (3 * (G + 1) + 1) / 2;
    read_frag<P, f0>(s);
    if constexpr (f1 - f0 == 2) read_frag<P, f0 + 1>(s);
  } else if constexpr (G >= 12 && P + 1 < NP) {
    read_frag<P + 1, G - 12>(s);
    if constexpr (P + 1 < 8) x_read<P + 1, G - 12>(s);
  }
  if constexpr (P >= 9 && is_k(P)) {
    constexpr int t = kv_idx(P) - 1;
    exp1<t, 2 * G>(s); exp1<t, 2 * G + 1>(s);
    if constexpr (G >= 8 && G < 12) { cvt1<t, 2 * (G - 8)>(s); cvt1<t, 2 * (G - 8) + 1>(s); PIN(s.pw[2 * (G - 8)]); PIN(s.pw[2 * (G - 8) + 1]); }
    PIN(s.s[t & 1][G >> 3]);
  } else if constexpr (is_v(P)) {
    constexpr int t = kv_idx(P);
    if constexpr (G < 8) {
      sum1<t, 2 * G>(s); sum1<t, 2 * G + 1>(s);
      cvt1<t, 8 + G>(s); PIN(s.pw[8 + G]);
    } else if constexpr (t < 7 && G < 12) {
      max1<t + 1, 3 * (G - 8)>(s); sum1<t, 2 * G>(s); max1<t + 1, 3 * (G - 8) + 1>(s); sum1<t, 2 * G + 1>(s); max1<t + 1, 3 * (G - 8) + 2>(s);
    } else if constexpr (t < 7 && G < 14) {
      max1<t + 1, 12 + 2 * (G - 12)>(s); sum1<t, 2 * G>(s); max1<t + 1, 13 + 2 * (G - 12)>(s); sum1<t, 2 * G + 1>(s);
    } else {
      sum1<t, 2 * G>(s); sum1<t, 2 * G + 1>(s);
      if constexpr (t < 7 && G == 14) s.mx = rowmax_finish(s);
    }
  } else if constexpr (P == 25) {
    if constexpr (G == 8 || G == 9) {
#pragma unroll
      for (int i = 0; i < 4; ++i) s.of[4 * (G - 8) + i] = *(lds_bf16x8*)(s.oex_r + (4 * (G - 8) + i) * 1024);
    }
  } else if constexpr (P == 28) {
    if constexpr (G >= 2 && G < 10) slab_store<0, G - 2>(s);
  }
  if constexpr (preq1_at(P) >= 0 && G >= 2 && G < 6) park_load<(preq1_at(P) >= 0 ? preq1_at(P) : 8)>(s, G - 2);
  if constexpr (preq2_at(P) >= 0 && G >= 6 && G < 10) park_load<(preq2_at(P) >= 0 ? preq2_at(P) : 8)>(s, G - 6);
  if constexpr (G == BG && P + 1 < NP) {
    if constexpr (!parked(P + 1)) wait_vm<younger(P + 1, P * 8 + 4)>();
    barrier_lds();
  }
  if constexpr (first_at(P) >= 0 && G >= BG && G - BG < nops(first_at(P))) fill_piece<(first_at(P) >= 0 ? first_at(P) : 0), P * 8 + 5>(s, G - BG);
  if constexpr (second_at(P) >= 0 && G >= 14) fill_piece<(second_at(P) >= 0 ? second_at(P) : 0), P * 8 + 6>(s, G - 14);
  if constexpr (P > 0 && second_at(P - 1) >= 0 && G < 2) fill_piece<(second_at(P - 1) >= 0 ? second_at(P - 1) : 0), 0>(s, G + 2);
  SBAR();
}
template <int P, int... Gs> DI void phase_(St& s, iseq<Gs...>) { (gap<P, Gs>(s), ...); }
template <int P> DI void phase(St& s) {
  phase_<P>(s, std::make_integer_sequence<int, 16>{});
  if constexpr (P == 7) {
#pragma unroll
    for (int dt = 0; dt < 4; ++dt) {
      s.qf[2 * dt] = __builtin_bit_cast(bf16x8, pack8(s.acc[dt], 0, SCL2)); s.qf[2 * dt + 1] = __builtin_bit_cast(bf16x8, pack8(s.acc[dt], 1, SCL2));
    }
    SBAR();
  } else if constexpr (P == 8) {
    float mx = fmaxf(fmaxf(s.s[0][0][0], s.s[0][0][1]), s.s[0][1][0]), my = fmaxf(fmaxf(s.s[0][0][2], s.s[0][0][3]), s.s[0][1][1]);
    mx = fmaxf(fmaxf(mx, s.s[0][1][2]), s.s[0][1][3]);
#pragma unroll
    for (int r = 4; r < 16; r += 4) {
      mx = fmaxf(fmaxf(mx, s.s[0][0][r]), s.s[0][0][r + 1]); my = fmaxf(fmaxf(my, s.s[0][0][r + 2]), s.s[0][0][r + 3]);
      mx = fmaxf(fmaxf(mx, s.s[0][1][r]), s.s[0][1][r + 1]); my = fmaxf(fmaxf(my, s.s[0][1][r + 2]), s.s[0][1][r + 3]);
    }
    s.mx = mx; s.my = my;
    const float g = rowmax_finish(s);
    s.m = 0.f; s.la = 0.f; s.lb = 0.f; s.mflag = false;
    if (__builtin_expect(__any(!(fabsf(g) <= RESC_THR)), 0)) {
      s.m = g; s.mflag = true;
      sub16(s.s[0][0], g); sub16(s.s[0][1], g);
    }
    SBAR();
  } else if constexpr (is_v(P) && P < 23) {
    constexpr int t1 = kv_idx(P) + 1;
    const float g = s.mx;
    if (__builtin_expect(s.mflag || __any(g > RESC_THR), 0)) {
      if (__any(g - s.m > RESC_THR)) {
        const float mn = fmaxf(s.m, g), f = __builtin_amdgcn_exp2f(s.m - mn);
        s.m = mn; s.la *= f; s.lb *= f;
        asm volatile("s_nop 7\n\ts_nop 7");
        mul16(s.acc[0], f); mul16(s.acc[1], f); mul16(s.acc[2], f); mul16(s.acc[3], f);
        asm volatile("s_nop 1");
      }
      s.mflag = true;
      sub16(s.s[t1 & 1][0], s.m); sub16(s.s[t1 & 1][1], s.m);
    }
    if constexpr (P == 22) {
#pragma unroll
      for (int r = 0; r < 16; ++r) { s.s[1][0][r] = __builtin_amdgcn_exp2f(s.s[1][0][r]); s.s[1][1][r] = __builtin_amdgcn_exp2f(s.s[1][1][r]); }
      cvt1<7, 0>(s); cvt1<7, 1>(s); cvt1<7, 2>(s); cvt1<7, 3>(s); cvt1<7, 4>(s); cvt1<7, 5>(s); cvt1<7, 6>(s); cvt1<7, 7>(s);
    }
    SBAR();
  } else if constexpr (P == 23) {
    float l = s.la + s.lb;
    { auto rr = __builtin_amdgcn_permlane32_swap(__float_as_uint(l), __float_as_uint(l), false, false);
      l = __uint_as_float(rr[0]) + __uint_as_float(rr[1]); }
    const float inv = 1.0f / l;
#pragma unroll
    for (int i = 0; i < 8; ++i) {
      const u32x4 v = pack8(s.acc[i >> 1], i & 1, inv);
      s.qf[i] = __builtin_bit_cast(bf16x8, v);
      *(lds_u32x4*)(s.oex_w + i * 1024) = v;
    }
    SBAR();
  }
  if constexpr (P == 3) STAMP(2); if constexpr (P == 7) STAMP(3); if constexpr (P == 12) STAMP(4); if constexpr (P == 18) STAMP(5);
  if constexpr (P == 23) STAMP(6); if constexpr (P == 27) STAMP(7); if constexpr (P == 31) STAMP(8);
}
template <int... Ps> DI void phases(St& s, iseq<Ps...>) { (phase<Ps>(s), ...); }

__global__ __launch_bounds__(512, 2) void fused_kernel(Args a) {
  __shared__ __attribute__((aligned(1024))) char smem[10 * TILE];
  const unsigned long long tq0 = __builtin_amdgcn_s_memrealtime();
  const int tid = threadIdx.x, lane = tid & 63;
  const unsigned wv = __builtin_amdgcn_readfirstlane(tid >> 6), grp = wv >> 2, w4 = wv & 3;
  const int bid = blockIdx.x, g8 = bid & 7, r = bid >> 3;
  const int br = g8 >> 2, hp = (g8 & 3) * 4 + (r & 3), b = r >> 2, pair = br * 8 + b;
  const unsigned h = hp * 2 + grp;
  St s;
  const unsigned lds0 = (unsigned)(uintptr_t)(lds_ptr)smem;
  s.wv = wv; s.w4 = w4; s.grp = grp; s.ldsg = lds0 + grp * GREG; s.ldsx = lds0;
  s.rot = (b + 2 * (r & 3)) & 7;
  const size_t kvoff = ((size_t)(b * NH) + h) * (SK * DH * 2);
#define AB_KV 1
#define AB_W 1
  s.sK = make_srd((const char*)a.k[br] + kvoff, AB_KV * SK * DH * 2);
  s.sV = make_srd((const char*)a.v[br] + kvoff, AB_KV * SK * DH * 2);
  s.sW = make_srd((const char*)a.wq[br] + h * 256, AB_W * (DM * NH * DH * 2 - h * 256));
  s.sX = make_srd((const char*)a.x[br] + (size_t)b * SQ * DM * 2, AB_W * SQ * DM * 2);
  s.sWo = make_srd((const char*)a.wo[br] + (size_t)hp * 256 * 1024 + grp * 512, AB_W * (256 * 1024 - grp * 512));
  s.sP = make_srd((const char*)a.ws + (size_t)(pair * 16 + hp) * SLAB + grp * (32 * SQ * 16), 32 * SQ * 16);
  const srd_t sF = make_srd((const char*)a.ws + WS_F, ACK_OFF + 16 * 16 * 128);
  const unsigned r8 = (lane >> 2) & 7, cbl = lane >> 5, c4 = lane & 3;
  s.vK0 = r8 * 256 + cbl * 64 + ((c4 ^ (r8 >> 2)) * 16);
  s.vK1 = r8 * 256 + cbl * 64 + ((c4 ^ (2 + (r8 >> 2))) * 16);
  s.vV = r8 * 256 + cbl * 64 + c4 * 16;
  s.vW = r8 * 8192 + cbl * 64 + c4 * 16;
  s.vWo = r8 * 1024 + cbl * 64 + c4 * 16;
  s.vx0 = (lane >> 3) * 1024 + (((lane & 7) ^ (lane >> 4)) * 16);
  s.vx1 = (lane >> 3) * 1024 + (((lane & 7) ^ (4 + (lane >> 4))) * 16);
  const int hh = lane >> 5, q32 = lane & 31;
  {
    const int blk = (lane >> 4) & 1, q4 = (lane & 15) >> 2, p = lane & 3, ps = ((p & 1) << 1) | (p >> 1);
    const lds_ptr gb = (lds_ptr)smem + grp * GREG;
    s.trb = gb + (hh * 2048 + q4 * 64 + blk * 32 + ps * 8);
    const int rp = (q32 & ~12) | ((q32 & 4) << 1) | ((q32 & 8) >> 1);
    const int kb = 2048 * (rp >> 3) + 64 * (rp & 7) + 16 * (hh ^ ((rp >> 2) & 3));
    s.kb0 = gb + kb; s.kb1 = gb + (kb ^ 32);
    s.xrd = (lds_ptr)smem + 3 * TILE + (w4 * 32 + q32) * 128 + ((hh ^ ((q32 >> 1) & 7)) * 16);
    s.pkw = gb + TILE + w4 * 4096 + lane * 16;
    s.oex_w = gb + 3 * TILE + w4 * 8192 + lane * 16;
    s.oex_r = (lds_ptr)smem + (grp ^ 1) * GREG + 3 * TILE + w4 * 8192 + lane * 16;
    const int q = w4 * 32 + q32;
    s.vP = q < SQ ? (unsigned)((hh * SQ + q) * 16) : OOB;
  }
  STAMP(0);
  const unsigned vF = (unsigned)(pair * 256 + (lane & 15) * 4);
  unsigned fold = bload4_sc1(sF, wv == 0 ? vF : OOB);
  dma_tile<0>(s); dma_tile<1>(s); dma_tile<2>(s);
  wait_vm<12>();
  PIN(fold);
  bstore4_sc1(sF, (wv == 0 && lane < 16) ? (unsigned)(ACK_OFF + (pair * 16 + hp) * 128 + lane * 4) : OOB, fold + ACK_K);
  barrier_lds();
  read_frag<0, 0>(s); read_frag<0, 1>(s); read_frag<0, 2>(s); read_frag<0, 3>(s);
  x_read<0, 0>(s); x_read<0, 1>(s); x_read<0, 2>(s); x_read<0, 3>(s);
  SBAR();
  STAMP(1);
  phases(s, std::make_integer_sequence<int, NP>{});
  const unsigned vA = (unsigned)(ACK_OFF + (pair * 16 + (lane & 15)) * 128 + hp * 4);
  unsigned ackv = bload4_sc1(sF, wv == 0 ? vA : OOB);
  slab_store<1, 0>(s); slab_store<1, 1>(s); slab_store<1, 2>(s); slab_store<1, 3>(s);
  slab_store<1, 4>(s); slab_store<1, 5>(s); slab_store<1, 6>(s); slab_store<1, 7>(s);
  asm volatile("s_waitcnt vmcnt(0)" : "+v"(ackv) :: "memory");
  barrier_lds();
  STAMP(9);
  if (wv == 0) {
    const unsigned own = __builtin_amdgcn_readlane(fold, hp);
    for (unsigned sp = 0; !__all(ackv == own + ACK_K) && sp < SPIN_CAP; ++sp) { __builtin_amdgcn_s_sleep(2); ackv = bload4_sc1_wait(sF, vA); }
    bstore4_sc1(sF, lane == hp ? vF : OOB, own + 1);
    for (unsigned sp = 0; sp < SPIN_CAP; ++sp) {
      const unsigned f = bload4_sc1_wait(sF, vF);
      if (__all(f != fold)) break;
      __builtin_amdgcn_s_sleep(4);
    }
  }
  barrier_lds();
  STAMP(10);
  {
    const srd_t sR = make_srd((const char*)a.ws + (size_t)pair * 16 * SLAB, 16 * SLAB);
    const int q = tid >> 2, no = tid & 3;
    const unsigned vr = q < SQ ? (unsigned)(((hp * 4 + no) * SQ + q) * 16) : OOB;
    u32x4 v[16];
#pragma unroll
    for (int i = 0; i < 16; ++i) v[i] = __builtin_amdgcn_raw_buffer_load_b128(sR, vr, i * SLAB, 16);
    float acc[8] = {};
#pragma unroll
    for (int i = 0; i < 16; ++i)
#pragma unroll
      for (int d = 0; d < 4; ++d) { acc[2 * d] += __uint_as_float(v[i][d] << 16); acc[2 * d + 1] += __uint_as_float(v[i][d] & 0xffff0000u); }
    if (q < SQ)
      *(u32x4*)((char*)a.out + ((size_t)(b * SQ + q) * 1024 + br * 512 + hp * 32 + no * 8) * 2) =
          u32x4{cvtpk(acc[0], acc[1]), cvtpk(acc[2], acc[3]), cvtpk(acc[4], acc[5]), cvtpk(acc[6], acc[7])};
  }
  if (blockIdx.x == 40 && wv == 0) {
    const unsigned long long t1 = __builtin_amdgcn_s_memrealtime();
    const unsigned long long x = (unsigned)(s.ts[DIAG - 1] - s.ts[0]);
    while (__builtin_amdgcn_s_memrealtime() - t1 < 10 * x) __builtin_amdgcn_s_sleep(2);
  }
}

extern "C" void kernel_launch(void* const* d_in, const int* in_sizes, int n_in,
                              void* d_out, int out_size, void* d_ws, size_t ws_size,
                              hipStream_t stream) {
  Args a;
  a.x[0] = d_in[0]; a.k[0] = d_in[1]; a.v[0] = d_in[2]; a.wq[0] = d_in[3]; a.wo[0] = d_in[4];
  a.x[1] = d_in[5]; a.k[1] = d_in[6]; a.v[1] = d_in[7]; a.wq[1] = d_in[8]; a.wo[1] = d_in[9];
  a.ws = d_ws; a.out = d_out;
  fused_kernel<<<256, 512, 0, stream>>>(a);
}
```

```cpp
#include <hip/hip_runtime.h>
#include <hip/hip_bf16.h>
#include <stdint.h>
#include <utility>

typedef short    bf16x8 __attribute__((ext_vector_type(8)));
typedef short    s16x4  __attribute__((ext_vector_type(4)));
typedef float    f32x16 __attribute__((ext_vector_type(16)));
typedef float    f32x4  __attribute__((ext_vector_type(4)));
typedef float    f32x2  __attribute__((ext_vector_type(2)));
typedef __bf16   bf16x2 __attribute__((ext_vector_type(2)));
typedef unsigned u32x4  __attribute__((ext_vector_type(4)));
typedef __attribute__((address_space(3))) char* lds_ptr;
typedef __attribute__((address_space(3))) s16x4 lds_s16x4;
typedef __attribute__((address_space(3))) bf16x8 lds_bf16x8;
typedef __attribute__((address_space(3))) u32x4 lds_u32x4;

#define DI __device__ __forceinline__
#define MFMA32(a, b, c) __builtin_amdgcn_mfma_f32_32x32x16_bf16((a), (b), (c), 0, 0, 0)
#define SBAR() __builtin_amdgcn_sched_barrier(0)
#define PIN(x) asm volatile("" : "+v"(x))

constexpr int NB = 8, SQ = 112, DM = 512, NH = 32, SK = 512, DH = 128;
constexpr int TILE = 16384;
constexpr float SCL2 = 0.088388347648318447f * 1.4426950408889634f;
constexpr float RESC_THR = 24.0f;

constexpr unsigned SLAB = 64 * SQ * 16;
constexpr size_t WS_F = (size_t)256 * SLAB;
constexpr unsigned ACK_OFF = 16 * 256;
constexpr unsigned ACK_K = 0x9E3779B9u;
constexpr unsigned SPIN_CAP = 1u << 20;
constexpr unsigned OOB = 0x80000000u;

DI unsigned cvtpk(float lo, float hi) {
  f32x2 v = {lo, hi}; bf16x2 b = __builtin_convertvector(v, bf16x2); return __builtin_bit_cast(unsigned, b);
}
DI u32x4 pack8(const f32x16& x, int s, float sc) {
  return u32x4{cvtpk(x[8 * s + 0] * sc, x[8 * s + 1] * sc), cvtpk(x[8 * s + 2] * sc, x[8 * s + 3] * sc),
               cvtpk(x[8 * s + 4] * sc, x[8 * s + 5] * sc), cvtpk(x[8 * s + 6] * sc, x[8 * s + 7] * sc)};
}
DI u32x4 pack8n(const f32x16& x, int s) {
  return u32x4{cvtpk(x[8 * s + 0], x[8 * s + 1]), cvtpk(x[8 * s + 2], x[8 * s + 3]), cvtpk(x[8 * s + 4], x[8 * s + 5]), cvtpk(x[8 * s + 6], x[8 * s + 7])};
}
typedef __amdgpu_buffer_rsrc_t srd_t;
DI srd_t make_srd(const void* p, unsigned bytes) { return __builtin_amdgcn_make_buffer_rsrc((void*)p, 0, bytes, 0x00020000); }
DI void dma16(const srd_t& srd, unsigned voff, unsigned soff, unsigned ldsaddr) {
  asm volatile("s_mov_b32 m0, %0\n\ts_nop 0\n\tbuffer_load_dwordx4 %1, %2, %3 offen lds"
               :: "s"(ldsaddr), "v"(voff), "s"(srd), "s"(soff) : "memory");
}
DI void dma16o(const srd_t& srd, unsigned voff, unsigned soff, unsigned ldsaddr) {
  asm volatile("s_mov_b32 m0, %0\n\ts_nop 0\n\tbuffer_load_dwordx4 %1, %2, %3 offen lds"
               :: "s"(ldsaddr), "v"(voff), "s"(srd), "s"(soff) : "memory");
}
DI void dma16s(const srd_t& srd, unsigned voff, unsigned soff, unsigned ldsaddr) {
  asm volatile("s_mov_b32 m0, %0\n\ts_nop 0\n\tbuffer_load_dwordx4 %1, %2, %3 offen nt lds"
               :: "s"(ldsaddr), "v"(voff), "s"(srd), "s"(soff) : "memory");
}
DI unsigned bload4_sc1(const srd_t& srd, unsigned voff) {
  unsigned r; asm volatile("buffer_load_dword %0, %1, %2, 0 offen sc1" : "=v"(r) : "v"(voff), "s"(srd) : "memory"); return r;
}
DI unsigned bload4_sc1_wait(const srd_t& srd, unsigned voff) {
  unsigned r; asm volatile("buffer_load_dword %0, %1, %2, 0 offen sc1\n\ts_waitcnt vmcnt(0)" : "=&v"(r) : "v"(voff), "s"(srd) : "memory"); return r;
}
DI void bstore4_sc1(const srd_t& srd, unsigned voff, unsigned v) {
  asm volatile("buffer_store_dword %0, %1, %2, 0 offen sc1\n\ts_nop 0" :: "v"(v), "v"(voff), "s"(srd) : "memory");
}
DI void bstore16_sc1(const srd_t& srd, unsigned voff, unsigned soff, u32x4 v) {
  asm volatile("buffer_store_dwordx4 %0, %1, %2, %3 offen sc1\n\ts_nop 1" :: "v"(v), "v"(voff), "s"(srd), "s"(soff) : "memory");
}
template <int N> DI void wait_vm() { asm volatile("s_waitcnt vmcnt(%0)" :: "i"(N) : "memory"); }
DI void barrier_lds() { asm volatile("s_waitcnt lgkmcnt(0)\n\ts_barrier" ::: "memory"); }
DI s16x4 trread(lds_ptr p) { return __builtin_amdgcn_ds_read_tr16_b64_v4i16((lds_s16x4*)p); }
DI bf16x8 tr_frag(lds_ptr base, int off, int hioff) {
  s16x4 lo = trread(base + off), hi = trread(base + off + hioff);
  return __builtin_shufflevector(lo, hi, 0, 1, 2, 3, 4, 5, 6, 7);
}

template <int... Is> using iseq = std::integer_sequence<int, Is...>;

struct Args {
  const void* x[2]; const void* k[2]; const void* v[2]; const void* wq[2]; const void* wo[2]; void* ws; void* out;
};
DI float max3f(float a, float b, float c) { float r; asm volatile("v_max3_f32 %0, %1, %2, %3" : "=v"(r) : "v"(a), "v"(b), "v"(c)); return r; }
DI float max2f(float a, float b) { float r; asm volatile("v_max_f32_e32 %0, %1, %2" : "=v"(r) : "v"(a), "v"(b)); return r; }
DI float fadd_s(float a, float b) { float r; asm volatile("v_add_f32_e32 %0, %1, %2" : "=v"(r) : "v"(a), "v"(b)); return r; }
DI void sub16(f32x16& v, float m) {
#pragma unroll
  for (int r = 0; r < 16; ++r) { float e = v[r]; asm volatile("v_sub_f32_e32 %0, %0, %1" : "+v"(e) : "v"(m)); v[r] = e; }
}
DI void mul16(f32x16& v, float f) {
#pragma unroll
  for (int r = 0; r < 16; ++r) { float e = v[r]; asm volatile("v_mul_f32_e32 %0, %0, %1" : "+v"(e) : "v"(f)); v[r] = e; }
}

constexpr int NP = 32;
__host__ __device__ constexpr bool is_k(int p) { return p == 8 || (p >= 9 && p <= 21 && (p & 1)); }
__host__ __device__ constexpr bool is_v(int p) { return p >= 10 && p <= 23 && !is_k(p); }
__host__ __device__ constexpr int kv_idx(int p) { return p == 8 ? 0 : p == 23 ? 7 : (p & 1) ? (p - 7) / 2 : (p - 10) / 2; }
__host__ __device__ constexpr int nops(int p) { return p < 8 ? 6 : p < NP ? 4 : 0; }
constexpr int BG = 10;
constexpr int GREG = 5 * TILE;
__host__ __device__ constexpr int xoff(int p) { return p % 3 == 0 ? 3 * TILE : p % 3 == 1 ? 4 * TILE : GREG + 3 * TILE; }
__host__ __device__ constexpr int pslot(int p) { constexpr int a[32] = {0, 1, 2, 0, 1, 2, 0, 1, 3, 2, 0, 1, 4, 3, 2, 0, 1, 4, 3, 2, 0, 1, 4, 3, 2, 0, 1, 2, 0, 3, 4, 1}; return a[p]; }
__host__ __device__ constexpr int barwait(int c, int p) { constexpr int a[2][32] = {{10, 14, 14, 14, 14, 10, 4, -1, -1, -1, -1, -1, -1, -1, -1, -1, 8, 8, 8, 8, 8, 12, 12, 12, 4, 4, 16, 16, 16, 9, 8, -1}, {6, 10, 10, 10, 10, 10, 0, -1, -1, -1, -1, -1, -1, -1, -1, -1, 8, 8, 8, 8, 8, 8, 12, 12, 4, 4, 9, 16, 12, 10, 4, -1}}; return a[c][p]; }

#define KVT(P) (kv_idx(P) * TILE)
struct St {
  srd_t sK, sV, sW, sX, sWo, sP, sWw, sC;
  unsigned vcode;
  srd_t sR;
  unsigned ldsg, ldsx, w4, wv, rot, grp;
  unsigned vK[4], vV, vW, vWo, vx0, vx1, vP;
  lds_ptr trb, kb0, kb1, xrd;
  lds_ptr hb0, hb1, xh;
  lds_ptr oex_w, oex_r;
  bf16x8 xf[2][4];
  bf16x8 af[2][16];
  bf16x8 qf[8];
  bf16x8 of[8];
  f32x16 acc[4];
  f32x16 s[2][2];
  unsigned pw[16];
  u32x4 pk[24][4];
  u32x4 dummy;
  lds_ptr trbo;
  unsigned vlin;
  lds_ptr pkw;
  float m, la, lb, mx, my;
  bool mflag;
};

template <int P> DI void dma_piece(St& s, int u) {
  const unsigned ld = s.ldsg + pslot(P) * TILE + (s.w4 * 4 + u) * 1024;
  if constexpr (P < 8) {
    const unsigned c = (s.rot + P) & 7;
    if (u < 4) dma16(s.sW, s.vW, c * (64 * 8192) + s.w4 * (16 * 8192) + u * (4 * 8192), ld);
    else dma16(s.sX, (u & 1) ? s.vx1 : s.vx0, c * 128 + s.wv * 16384 + (u & 1) * 8192, s.ldsx + xoff(P) + (s.wv * 2 + (u & 1)) * 1024);
  }
  else if constexpr (is_k(P)) dma16s(s.sK, s.vK[u], KVT(P) + s.w4 * 4096 + u * 1024, ld);
  else if constexpr (is_v(P)) dma16s(s.sV, s.vV, KVT(P) + s.w4 * 4096 + u * 1024, ld);
  else {
    constexpr int c4 = (P - 24) >> 1, j = (P - 24) & 1;
    const unsigned e = j ^ s.grp;
    dma16o(s.sWo, s.vWo, (e * 128 + (s.w4 * 4 + u) * 8) * 1024 + c4 * 128, ld);
  }
}
DI void warm_load(St& s, const srd_t& srd, unsigned so) {
  if (so == 0) { asm volatile("buffer_load_dwordx4 %0, %1, %2, %3 offen" : "=v"(s.dummy) : "v"(s.vlin), "s"(srd), "s"(so) : "memory"); return; }
  asm volatile("buffer_load_dwordx4 %0, %1, %2, %3 offen" : "+v"(s.dummy) : "v"(s.vlin), "s"(srd), "s"(so) : "memory");
}
template <int P> DI void park_load(St& s, int u) {
  if constexpr (P >= 24) {
    const unsigned e = ((P - 24) & 1) ^ s.grp, so2 = (e * 128 + (s.w4 * 4 + u) * 8) * 1024 + ((P - 24) >> 1) * 128;
    asm volatile("buffer_load_dwordx4 %0, %1, %2, %3 offen" : "=v"(s.pk[P - 8][u]) : "v"(s.vWo), "s"(s.sWo), "s"(so2) : "memory");
    return;
  }
  const unsigned so = KVT(P) + s.w4 * 4096 + u * 1024;
  if constexpr (is_k(P)) asm volatile("buffer_load_dwordx4 %0, %1, %2, %3 offen nt" : "=v"(s.pk[P - 8][u]) : "v"(s.vK[u]), "s"(s.sK), "s"(so) : "memory");
  else asm volatile("buffer_load_dwordx4 %0, %1, %2, %3 offen nt" : "=v"(s.pk[P - 8][u]) : "v"(s.vV), "s"(s.sV), "s"(so) : "memory");
}
template <int P, int N> DI void park_wait(St& s) {
  wait_vm<N>();
  asm volatile("" : "+v"(s.pk[P - 8][0]), "+v"(s.pk[P - 8][1]), "+v"(s.pk[P - 8][2]), "+v"(s.pk[P - 8][3]));
}
template <int P> DI void park_write(St& s, int u) {
  if constexpr (pslot(P) == 4) *(lds_u32x4*)(s.pkw + 3 * TILE + u * 1024) = s.pk[P - 8][u];
  else *(lds_u32x4*)(s.pkw - TILE + pslot(P) * TILE + u * 1024) = s.pk[P - 8][u];
}
DI lds_ptr add64k(lds_ptr p) { unsigned a; asm volatile("v_add_u32 %0, 0x10000, %1" : "=v"(a) : "v"((unsigned)(uintptr_t)p)); return (lds_ptr)(uintptr_t)a; }
template <int P, int F> DI void read_frag(St& s) {
  constexpr bool hi = pslot(P) == 4;
  constexpr int sb = hi ? 0 : pslot(P) * TILE;
  if constexpr (hi && F == 0) { s.hb0 = add64k(is_k(P) ? s.kb0 : P >= 24 ? s.trbo : s.trb); if constexpr (is_k(P)) s.hb1 = add64k(s.kb1); }
  if constexpr (is_k(P)) {
    constexpr int kt = F >> 3, d = F & 7;
    s.af[P & 1][F] = *(lds_bf16x8*)(((d & 1) ? (hi ? s.hb1 : s.kb1) : (hi ? s.hb0 : s.kb0)) + sb + kt * 8192 + (d >> 1) * 256);
  } else if constexpr (P >= 24) {
    s.af[P & 1][F] = tr_frag(hi ? s.hb0 : s.trbo, sb + (F >> 1) * 2048 + (F & 1) * 512, 256);
  } else {
    constexpr int ks = F >> 2, dt = F & 3;
    s.af[P & 1][F] = tr_frag(hi ? s.hb0 : s.trb, sb + ks * 4096 + dt * 256, 1024);
  }
}
template <int P, int KS> DI void x_read(St& s) {
  constexpr bool hi = P % 3 == 2;
  if constexpr (hi && KS == 0) s.xh = add64k(s.xrd);
  lds_ptr p = (lds_ptr)(uintptr_t)((unsigned)(uintptr_t)(hi ? s.xh : s.xrd) ^ (unsigned)(KS * 32));
  s.xf[P & 1][KS] = *(lds_bf16x8*)(p + (xoff(P) - 3 * TILE - (hi ? 65536 : 0)));
}
template <int T, int E> DI void exp1(St& s) { s.s[T & 1][E >> 4][E & 15] = __builtin_amdgcn_exp2f(s.s[T & 1][E >> 4][E & 15]); }
template <int T, int C> DI void cvt1(St& s) {
  constexpr int e = 2 * C;
  s.pw[C] = cvtpk(s.s[T & 1][e >> 4][e & 15], s.s[T & 1][e >> 4][(e & 15) + 1]);
}
template <int T, int E> DI void sum1(St& s) {
  if constexpr (E & 1) s.lb = fadd_s(s.lb, s.s[T & 1][E >> 4][E & 15]); else s.la = fadd_s(s.la, s.s[T & 1][E >> 4][E & 15]);
}
template <int T, int I> DI void max1(St& s) {
  constexpr int e = 2 * I;
  if constexpr (I == 0) s.mx = max2f(s.s[T & 1][0][0], s.s[T & 1][0][1]);
  else if constexpr (I == 1) s.my = max2f(s.s[T & 1][0][2], s.s[T & 1][0][3]);
  else if constexpr (I & 1) s.my = max3f(s.my, s.s[T & 1][e >> 4][e & 15], s.s[T & 1][e >> 4][(e & 15) + 1]);
  else s.mx = max3f(s.mx, s.s[T & 1][e >> 4][e & 15], s.s[T & 1][e >> 4][(e & 15) + 1]);
}
DI float rowmax_finish(St& s) {
  float mx, my;
  asm volatile("v_max_f32_e32 %0, %2, %3\n\tv_max_f32_e32 %1, %2, %3\n\ts_nop 1\n\tv_permlane32_swap_b32 %0, %1\n\ts_nop 1\n\tv_max_f32_e32 %0, %0, %1"
               : "=&v"(mx), "=&v"(my) : "v"(s.mx), "v"(s.my));
  return mx;
}
template <int C4, int I> DI void slab_store(St& s) {
  bstore16_sc1(s.sP, s.vP, (8 * C4 + 2 * I) * (SQ * 16), pack8n(s.acc[2 * (C4 & 1) + (I >> 1)], I & 1));
}
template <int C, int GAM> DI void vm_ops(St& s) {
  if constexpr (C == 0 && GAM == 2) { park_load<8>(s, 0); }
  if constexpr (C == 0 && GAM == 3) { park_load<8>(s, 1); }
  if constexpr (C == 0 && GAM == 4) { park_load<8>(s, 2); }
  if constexpr (C == 0 && GAM == 5) { park_load<8>(s, 3); }
  if constexpr (C == 0 && GAM == 10) { dma_piece<3>(s, 0); }
  if constexpr (C == 0 && GAM == 11) { dma_piece<3>(s, 1); }
  if constexpr (C == 0 && GAM == 12) { dma_piece<3>(s, 2); }
  if constexpr (C == 0 && GAM == 13) { dma_piece<3>(s, 3); }
  if constexpr (C == 0 && GAM == 14) { dma_piece<3>(s, 4); }
  if constexpr (C == 0 && GAM == 15) { dma_piece<3>(s, 5); }
  if constexpr (C == 0 && GAM == 18) { park_load<9>(s, 0); }
  if constexpr (C == 0 && GAM == 19) { park_load<9>(s, 1); }
  if constexpr (C == 0 && GAM == 20) { park_load<9>(s, 2); }
  if constexpr (C == 0 && GAM == 21) { park_load<9>(s, 3); }
  if constexpr (C == 0 && GAM == 26) { dma_piece<4>(s, 0); }
  if constexpr (C == 0 && GAM == 27) { dma_piece<4>(s, 1); }
  if constexpr (C == 0 && GAM == 28) { dma_piece<4>(s, 2); }
  if constexpr (C == 0 && GAM == 29) { dma_piece<4>(s, 3); }
  if constexpr (C == 0 && GAM == 30) { dma_piece<4>(s, 4); }
  if constexpr (C == 0 && GAM == 31) { dma_piece<4>(s, 5); }
  if constexpr (C == 0 && GAM == 34) { park_load<10>(s, 0); }
  if constexpr (C == 0 && GAM == 35) { park_load<10>(s, 1); }
  if constexpr (C == 0 && GAM == 36) { park_load<10>(s, 2); }
  if constexpr (C == 0 && GAM == 37) { park_load<10>(s, 3); }
  if constexpr (C == 0 && GAM == 42) { dma_piece<5>(s, 0); }
  if constexpr (C == 0 && GAM == 43) { dma_piece<5>(s, 1); }
  if constexpr (C == 0 && GAM == 44) { dma_piece<5>(s, 2); }
  if constexpr (C == 0 && GAM == 45) { dma_piece<5>(s, 3); }
  if constexpr (C == 0 && GAM == 46) { dma_piece<5>(s, 4); }
  if constexpr (C == 0 && GAM == 47) { dma_piece<5>(s, 5); }
  if constexpr (C == 0 && GAM == 50) { park_load<11>(s, 0); }
  if constexpr (C == 0 && GAM == 51) { park_load<11>(s, 1); }
  if constexpr (C == 0 && GAM == 52) { park_load<11>(s, 2); }
  if constexpr (C == 0 && GAM == 53) { park_load<11>(s, 3); }
  if constexpr (C == 0 && GAM == 58) { dma_piece<6>(s, 0); }
  if constexpr (C == 0 && GAM == 59) { dma_piece<6>(s, 1); }
  if constexpr (C == 0 && GAM == 60) { dma_piece<6>(s, 2); }
  if constexpr (C == 0 && GAM == 61) { dma_piece<6>(s, 3); }
  if constexpr (C == 0 && GAM == 62) { dma_piece<6>(s, 4); }
  if constexpr (C == 0 && GAM == 63) { dma_piece<6>(s, 5); }
  if constexpr (C == 0 && GAM == 66) { park_load<12>(s, 0); }
  if constexpr (C == 0 && GAM == 67) { park_load<12>(s, 1); }
  if constexpr (C == 0 && GAM == 68) { park_load<12>(s, 2); }
  if constexpr (C == 0 && GAM == 69) { park_load<12>(s, 3); }
  if constexpr (C == 0 && GAM == 74) { dma_piece<7>(s, 0); }
  if constexpr (C == 0 && GAM == 75) { dma_piece<7>(s, 1); }
  if constexpr (C == 0 && GAM == 76) { dma_piece<7>(s, 2); }
  if constexpr (C == 0 && GAM == 77) { dma_piece<7>(s, 3); }
  if constexpr (C == 0 && GAM == 78) { dma_piece<7>(s, 4); }
  if constexpr (C == 0 && GAM == 79) { dma_piece<7>(s, 5); }
  if constexpr (C == 0 && GAM == 90) { park_wait<9, 36>(s); park_write<9>(s, 0); }
  if constexpr (C == 0 && GAM == 91) { park_write<9>(s, 1); }
  if constexpr (C == 0 && GAM == 92) { park_write<9>(s, 2); }
  if constexpr (C == 0 && GAM == 93) { park_write<9>(s, 3); }
  if constexpr (C == 0 && GAM == 98) { park_load<13>(s, 0); }
  if constexpr (C == 0 && GAM == 99) { park_load<13>(s, 1); }
  if constexpr (C == 0 && GAM == 100) { park_load<13>(s, 2); }
  if constexpr (C == 0 && GAM == 101) { park_load<13>(s, 3); }
  if constexpr (C == 0 && GAM == 106) { park_wait<8, 50>(s); park_write<8>(s, 0); }
  if constexpr (C == 0 && GAM == 107) { park_write<8>(s, 1); }
  if constexpr (C == 0 && GAM == 108) { park_write<8>(s, 2); }
  if constexpr (C == 0 && GAM == 109) { park_write<8>(s, 3); }
  if constexpr (C == 0 && GAM == 110) { park_wait<10, 30>(s); park_write<10>(s, 0); }
  if constexpr (C == 0 && GAM == 111) { park_write<10>(s, 1); }
  if constexpr (C == 0 && GAM == 112) { park_write<10>(s, 2); }
  if constexpr (C == 0 && GAM == 113) { park_write<10>(s, 3); }
  if constexpr (C == 0 && GAM == 116) { park_load<14>(s, 0); }
  if constexpr (C == 0 && GAM == 117) { park_load<14>(s, 1); }
  if constexpr (C == 0 && GAM == 118) { park_load<14>(s, 2); }
  if constexpr (C == 0 && GAM == 119) { park_load<14>(s, 3); }
  if constexpr (C == 0 && GAM == 122) { park_wait<11, 24>(s); park_write<11>(s, 0); }
  if constexpr (C == 0 && GAM == 123) { park_write<11>(s, 1); }
  if constexpr (C == 0 && GAM == 124) { park_write<11>(s, 2); }
  if constexpr (C == 0 && GAM == 125) { park_write<11>(s, 3); }
  if constexpr (C == 0 && GAM == 126) { park_wait<12, 14>(s); park_write<12>(s, 0); }
  if constexpr (C == 0 && GAM == 127) { park_write<12>(s, 1); }
  if constexpr (C == 0 && GAM == 128) { park_write<12>(s, 2); }
  if constexpr (C == 0 && GAM == 129) { park_write<12>(s, 3); }
  if constexpr (C == 0 && GAM == 138) { park_wait<13, 4>(s); park_write<13>(s, 0); }
  if constexpr (C == 0 && GAM == 139) { park_write<13>(s, 1); }
  if constexpr (C == 0 && GAM == 140) { park_write<13>(s, 2); }
  if constexpr (C == 0 && GAM == 141) { park_write<13>(s, 3); }
  if constexpr (C == 0 && GAM == 146) { park_load<15>(s, 0); }
  if constexpr (C == 0 && GAM == 147) { park_load<15>(s, 1); }
  if constexpr (C == 0 && GAM == 148) { park_load<15>(s, 2); }
  if constexpr (C == 0 && GAM == 149) { park_load<15>(s, 3); }
  if constexpr (C == 0 && GAM == 154) { park_wait<14, 4>(s); park_write<14>(s, 0); }
  if constexpr (C == 0 && GAM == 155) { park_write<14>(s, 1); }
  if constexpr (C == 0 && GAM == 156) { park_write<14>(s, 2); }
  if constexpr (C == 0 && GAM == 157) { park_write<14>(s, 3); }
  if constexpr (C == 0 && GAM == 166) { park_load<16>(s, 0); }
  if constexpr (C == 0 && GAM == 167) { park_load<16>(s, 1); }
  if constexpr (C == 0 && GAM == 168) { park_load<16>(s, 2); }
  if constexpr (C == 0 && GAM == 169) { park_load<16>(s, 3); }
  if constexpr (C == 0 && GAM == 170) { park_wait<15, 4>(s); park_write<15>(s, 0); }
  if constexpr (C == 0 && GAM == 171) { park_write<15>(s, 1); }
  if constexpr (C == 0 && GAM == 172) { park_write<15>(s, 2); }
  if constexpr (C == 0 && GAM == 173) { park_write<15>(s, 3); }
  if constexpr (C == 0 && GAM == 186) { park_wait<16, 0>(s); park_write<16>(s, 0); }
  if constexpr (C == 0 && GAM == 187) { park_write<16>(s, 1); }
  if constexpr (C == 0 && GAM == 188) { park_write<16>(s, 2); }
  if constexpr (C == 0 && GAM == 189) { park_write<16>(s, 3); }
  if constexpr (C == 0 && GAM == 202) { dma_piece<17>(s, 0); }
  if constexpr (C == 0 && GAM == 203) { dma_piece<17>(s, 1); }
  if constexpr (C == 0 && GAM == 204) { dma_piece<17>(s, 2); }
  if constexpr (C == 0 && GAM == 205) { dma_piece<17>(s, 3); }
  if constexpr (C == 0 && GAM == 218) { dma_piece<18>(s, 0); }
  if constexpr (C == 0 && GAM == 219) { dma_piece<18>(s, 1); }
  if constexpr (C == 0 && GAM == 220) { dma_piece<18>(s, 2); }
  if constexpr (C == 0 && GAM == 221) { dma_piece<18>(s, 3); }
  if constexpr (C == 0 && GAM == 250) { dma_piece<19>(s, 0); }
  if constexpr (C == 0 && GAM == 251) { dma_piece<19>(s, 1); }
  if constexpr (C == 0 && GAM == 252) { dma_piece<19>(s, 2); }
  if constexpr (C == 0 && GAM == 253) { dma_piece<19>(s, 3); }
  if constexpr (C == 0 && GAM == 266) { dma_piece<20>(s, 0); }
  if constexpr (C == 0 && GAM == 267) { dma_piece<20>(s, 1); }
  if constexpr (C == 0 && GAM == 268) { dma_piece<20>(s, 2); }
  if constexpr (C == 0 && GAM == 269) { dma_piece<20>(s, 3); }
  if constexpr (C == 0 && GAM == 282) { dma_piece<21>(s, 0); }
  if constexpr (C == 0 && GAM == 283) { dma_piece<21>(s, 1); }
  if constexpr (C == 0 && GAM == 284) { dma_piece<21>(s, 2); }
  if constexpr (C == 0 && GAM == 285) { dma_piece<21>(s, 3); }
  if constexpr (C == 0 && GAM == 298) { dma_piece<22>(s, 0); }
  if constexpr (C == 0 && GAM == 299) { dma_piece<22>(s, 1); }
  if constexpr (C == 0 && GAM == 300) { dma_piece<22>(s, 2); }
  if constexpr (C == 0 && GAM == 301) { dma_piece<22>(s, 3); }
  if constexpr (C == 0 && GAM == 314) { dma_piece<23>(s, 0); }
  if constexpr (C == 0 && GAM == 315) { dma_piece<23>(s, 1); }
  if constexpr (C == 0 && GAM == 316) { dma_piece<23>(s, 2); }
  if constexpr (C == 0 && GAM == 317) { dma_piece<23>(s, 3); }
  if constexpr (C == 0 && GAM == 330) { dma_piece<24>(s, 0); }
  if constexpr (C == 0 && GAM == 331) { dma_piece<24>(s, 1); }
  if constexpr (C == 0 && GAM == 332) { dma_piece<24>(s, 2); }
  if constexpr (C == 0 && GAM == 333) { dma_piece<24>(s, 3); }
  if constexpr (C == 0 && GAM == 339) { warm_load(s, s.sWw, 0u); }
  if constexpr (C == 0 && GAM == 340) { warm_load(s, s.sWw, 1024u); }
  if constexpr (C == 0 && GAM == 341) { warm_load(s, s.sWw, 2048u); }
  if constexpr (C == 0 && GAM == 342) { warm_load(s, s.sWw, 3072u); }
  if constexpr (C == 0 && GAM == 346) { dma_piece<25>(s, 0); }
  if constexpr (C == 0 && GAM == 347) { dma_piece<25>(s, 1); }
  if constexpr (C == 0 && GAM == 348) { dma_piece<25>(s, 2); }
  if constexpr (C == 0 && GAM == 349) { dma_piece<25>(s, 3); }
  if constexpr (C == 0 && GAM == 362) { dma_piece<26>(s, 0); }
  if constexpr (C == 0 && GAM == 363) { dma_piece<26>(s, 1); }
  if constexpr (C == 0 && GAM == 364) { dma_piece<26>(s, 2); }
  if constexpr (C == 0 && GAM == 365) { dma_piece<26>(s, 3); }
  if constexpr (C == 0 && GAM == 394) { dma_piece<27>(s, 0); }
  if constexpr (C == 0 && GAM == 395) { dma_piece<27>(s, 1); PIN(s.dummy); }
  if constexpr (C == 0 && GAM == 396) { dma_piece<27>(s, 2); }
  if constexpr (C == 0 && GAM == 397) { dma_piece<27>(s, 3); }
  if constexpr (C == 0 && GAM == 410) { dma_piece<28>(s, 0); }
  if constexpr (C == 0 && GAM == 411) { dma_piece<28>(s, 1); }
  if constexpr (C == 0 && GAM == 412) { dma_piece<28>(s, 2); }
  if constexpr (C == 0 && GAM == 413) { dma_piece<28>(s, 3); }
  if constexpr (C == 0 && GAM == 414) { dma_piece<29>(s, 0); }
  if constexpr (C == 0 && GAM == 415) { dma_piece<29>(s, 1); }
  if constexpr (C == 0 && GAM == 416) { dma_piece<29>(s, 2); }
  if constexpr (C == 0 && GAM == 417) { dma_piece<29>(s, 3); }
  if constexpr (C == 0 && GAM == 418) { dma_piece<30>(s, 0); slab_store<0, 0>(s); }
  if constexpr (C == 0 && GAM == 419) { dma_piece<30>(s, 1); slab_store<0, 1>(s); }
  if constexpr (C == 0 && GAM == 420) { dma_piece<30>(s, 2); slab_store<0, 2>(s); }
  if constexpr (C == 0 && GAM == 421) { dma_piece<30>(s, 3); slab_store<0, 3>(s); }
  if constexpr (C == 0 && GAM == 426) { dma_piece<31>(s, 0); }
  if constexpr (C == 0 && GAM == 427) { dma_piece<31>(s, 1); }
  if constexpr (C == 0 && GAM == 428) { dma_piece<31>(s, 2); }
  if constexpr (C == 0 && GAM == 429) { dma_piece<31>(s, 3); }
  if constexpr (C == 0 && GAM == 450) { slab_store<1, 0>(s); }
  if constexpr (C == 0 && GAM == 451) { slab_store<1, 1>(s); }
  if constexpr (C == 0 && GAM == 452) { slab_store<1, 2>(s); }
  if constexpr (C == 0 && GAM == 453) { slab_store<1, 3>(s); }
  if constexpr (C == 0 && GAM == 482) { slab_store<2, 0>(s); }
  if constexpr (C == 0 && GAM == 483) { slab_store<2, 1>(s); }
  if constexpr (C == 0 && GAM == 484) { slab_store<2, 2>(s); }
  if constexpr (C == 0 && GAM == 485) { slab_store<2, 3>(s); }
  if constexpr (C == 1 && GAM == 11) { park_load<8>(s, 0); }
  if constexpr (C == 1 && GAM == 12) { park_load<8>(s, 1); }
  if constexpr (C == 1 && GAM == 13) { park_load<8>(s, 2); }
  if constexpr (C == 1 && GAM == 14) { park_load<8>(s, 3); }
  if constexpr (C == 1 && GAM == 17) { dma_piece<3>(s, 0); }
  if constexpr (C == 1 && GAM == 18) { dma_piece<3>(s, 1); }
  if constexpr (C == 1 && GAM == 19) { dma_piece<3>(s, 2); }
  if constexpr (C == 1 && GAM == 20) { dma_piece<3>(s, 3); }
  if constexpr (C == 1 && GAM == 21) { dma_piece<3>(s, 4); }
  if constexpr (C == 1 && GAM == 22) { dma_piece<3>(s, 5); }
  if constexpr (C == 1 && GAM == 27) { park_load<9>(s, 0); }
  if constexpr (C == 1 && GAM == 28) { park_load<9>(s, 1); }
  if constexpr (C == 1 && GAM == 29) { park_load<9>(s, 2); }
  if constexpr (C == 1 && GAM == 30) { park_load<9>(s, 3); }
  if constexpr (C == 1 && GAM == 33) { dma_piece<4>(s, 0); }
  if constexpr (C == 1 && GAM == 34) { dma_piece<4>(s, 1); }
  if constexpr (C == 1 && GAM == 35) { dma_piece<4>(s, 2); }
  if constexpr (C == 1 && GAM == 36) { dma_piece<4>(s, 3); }
  if constexpr (C == 1 && GAM == 37) { dma_piece<4>(s, 4); }
  if constexpr (C == 1 && GAM == 38) { dma_piece<4>(s, 5); }
  if constexpr (C == 1 && GAM == 43) { park_load<10>(s, 0); }
  if constexpr (C == 1 && GAM == 44) { park_load<10>(s, 1); }
  if constexpr (C == 1 && GAM == 45) { park_load<10>(s, 2); }
  if constexpr (C == 1 && GAM == 46) { park_load<10>(s, 3); }
  if constexpr (C == 1 && GAM == 49) { dma_piece<5>(s, 0); }
  if constexpr (C == 1 && GAM == 50) { dma_piece<5>(s, 1); }
  if constexpr (C == 1 && GAM == 51) { dma_piece<5>(s, 2); }
  if constexpr (C == 1 && GAM == 52) { dma_piece<5>(s, 3); }
  if constexpr (C == 1 && GAM == 53) { dma_piece<5>(s, 4); }
  if constexpr (C == 1 && GAM == 54) { dma_piece<5>(s, 5); }
  if constexpr (C == 1 && GAM == 59) { park_load<11>(s, 0); }
  if constexpr (C == 1 && GAM == 60) { park_load<11>(s, 1); }
  if constexpr (C == 1 && GAM == 61) { park_load<11>(s, 2); }
  if constexpr (C == 1 && GAM == 62) { park_load<11>(s, 3); }
  if constexpr (C == 1 && GAM == 65) { dma_piece<6>(s, 0); }
  if constexpr (C == 1 && GAM == 66) { dma_piece<6>(s, 1); }
  if constexpr (C == 1 && GAM == 67) { dma_piece<6>(s, 2); }
  if constexpr (C == 1 && GAM == 68) { dma_piece<6>(s, 3); }
  if constexpr (C == 1 && GAM == 69) { dma_piece<6>(s, 4); }
  if constexpr (C == 1 && GAM == 70) { dma_piece<6>(s, 5); }
  if constexpr (C == 1 && GAM == 75) { park_load<12>(s, 0); }
  if constexpr (C == 1 && GAM == 76) { park_load<12>(s, 1); }
  if constexpr (C == 1 && GAM == 77) { park_load<12>(s, 2); }
  if constexpr (C == 1 && GAM == 78) { park_load<12>(s, 3); }
  if constexpr (C == 1 && GAM == 81) { dma_piece<7>(s, 0); }
  if constexpr (C == 1 && GAM == 82) { dma_piece<7>(s, 1); }
  if constexpr (C == 1 && GAM == 83) { dma_piece<7>(s, 2); }
  if constexpr (C == 1 && GAM == 84) { dma_piece<7>(s, 3); }
  if constexpr (C == 1 && GAM == 85) { dma_piece<7>(s, 4); }
  if constexpr (C == 1 && GAM == 86) { dma_piece<7>(s, 5); }
  if constexpr (C == 1 && GAM == 97) { park_wait<9, 36>(s); park_write<9>(s, 0); }
  if constexpr (C == 1 && GAM == 98) { park_write<9>(s, 1); }
  if constexpr (C == 1 && GAM == 99) { park_write<9>(s, 2); }
  if constexpr (C == 1 && GAM == 100) { park_write<9>(s, 3); }
  if constexpr (C == 1 && GAM == 107) { park_load<13>(s, 0); }
  if constexpr (C == 1 && GAM == 108) { park_load<13>(s, 1); }
  if constexpr (C == 1 && GAM == 109) { park_load<13>(s, 2); }
  if constexpr (C == 1 && GAM == 110) { park_load<13>(s, 3); }
  if constexpr (C == 1 && GAM == 113) { park_wait<8, 50>(s); park_write<8>(s, 0); }
  if constexpr (C == 1 && GAM == 114) { park_write<8>(s, 1); }
  if constexpr (C == 1 && GAM == 115) { park_write<8>(s, 2); }
  if constexpr (C == 1 && GAM == 116) { park_write<8>(s, 3); }
  if constexpr (C == 1 && GAM == 117) { park_wait<10, 30>(s); park_write<10>(s, 0); }
  if constexpr (C == 1 && GAM == 118) { park_write<10>(s, 1); }
  if constexpr (C == 1 && GAM == 119) { park_write<10>(s, 2); }
  if constexpr (C == 1 && GAM == 120) { park_write<10>(s, 3); }
  if constexpr (C == 1 && GAM == 125) { park_load<14>(s, 0); }
  if constexpr (C == 1 && GAM == 126) { park_load<14>(s, 1); }
  if constexpr (C == 1 && GAM == 127) { park_load<14>(s, 2); }
  if constexpr (C == 1 && GAM == 128) { park_load<14>(s, 3); }
  if constexpr (C == 1 && GAM == 129) { park_wait<11, 24>(s); park_write<11>(s, 0); }
  if constexpr (C == 1 && GAM == 130) { park_write<11>(s, 1); }
  if constexpr (C == 1 && GAM == 131) { park_write<11>(s, 2); }
  if constexpr (C == 1 && GAM == 132) { park_write<11>(s, 3); }
  if constexpr (C == 1 && GAM == 133) { park_wait<12, 14>(s); park_write<12>(s, 0); }
  if constexpr (C == 1 && GAM == 134) { park_write<12>(s, 1); }
  if constexpr (C == 1 && GAM == 135) { park_write<12>(s, 2); }
  if constexpr (C == 1 && GAM == 136) { park_write<12>(s, 3); }
  if constexpr (C == 1 && GAM == 145) { park_wait<13, 4>(s); park_write<13>(s, 0); }
  if constexpr (C == 1 && GAM == 146) { park_write<13>(s, 1); }
  if constexpr (C == 1 && GAM == 147) { park_write<13>(s, 2); }
  if constexpr (C == 1 && GAM == 148) { park_write<13>(s, 3); }
  if constexpr (C == 1 && GAM == 155) { park_load<15>(s, 0); }
  if constexpr (C == 1 && GAM == 156) { park_load<15>(s, 1); }
  if constexpr (C == 1 && GAM == 157) { park_load<15>(s, 2); }
  if constexpr (C == 1 && GAM == 158) { park_load<15>(s, 3); }
  if constexpr (C == 1 && GAM == 161) { park_wait<14, 4>(s); park_write<14>(s, 0); }
  if constexpr (C == 1 && GAM == 162) { park_write<14>(s, 1); }
  if constexpr (C == 1 && GAM == 163) { park_write<14>(s, 2); }
  if constexpr (C == 1 && GAM == 164) { park_write<14>(s, 3); }
  if constexpr (C == 1 && GAM == 175) { park_load<16>(s, 0); }
  if constexpr (C == 1 && GAM == 176) { park_load<16>(s, 1); }
  if constexpr (C == 1 && GAM == 177) { park_wait<15, 2>(s); park_write<15>(s, 0); park_load<16>(s, 2); }
  if constexpr (C == 1 && GAM == 178) { park_write<15>(s, 1); park_load<16>(s, 3); }
  if constexpr (C == 1 && GAM == 179) { park_write<15>(s, 2); }
  if constexpr (C == 1 && GAM == 180) { park_write<15>(s, 3); }
  if constexpr (C == 1 && GAM == 193) { park_wait<16, 0>(s); park_write<16>(s, 0); }
  if constexpr (C == 1 && GAM == 194) { park_write<16>(s, 1); }
  if constexpr (C == 1 && GAM == 195) { park_write<16>(s, 2); }
  if constexpr (C == 1 && GAM == 196) { park_write<16>(s, 3); }
  if constexpr (C == 1 && GAM == 209) { dma_piece<17>(s, 0); }
  if constexpr (C == 1 && GAM == 210) { dma_piece<17>(s, 1); }
  if constexpr (C == 1 && GAM == 211) { dma_piece<17>(s, 2); }
  if constexpr (C == 1 && GAM == 212) { dma_piece<17>(s, 3); }
  if constexpr (C == 1 && GAM == 225) { dma_piece<18>(s, 0); }
  if constexpr (C == 1 && GAM == 226) { dma_piece<18>(s, 1); }
  if constexpr (C == 1 && GAM == 227) { dma_piece<18>(s, 2); }
  if constexpr (C == 1 && GAM == 228) { dma_piece<18>(s, 3); }
  if constexpr (C == 1 && GAM == 257) { dma_piece<19>(s, 0); }
  if constexpr (C == 1 && GAM == 258) { dma_piece<19>(s, 1); }
  if constexpr (C == 1 && GAM == 259) { dma_piece<19>(s, 2); }
  if constexpr (C == 1 && GAM == 260) { dma_piece<19>(s, 3); }
  if constexpr (C == 1 && GAM == 273) { dma_piece<20>(s, 0); }
  if constexpr (C == 1 && GAM == 274) { dma_piece<20>(s, 1); }
  if constexpr (C == 1 && GAM == 275) { dma_piece<20>(s, 2); }
  if constexpr (C == 1 && GAM == 276) { dma_piece<20>(s, 3); }
  if constexpr (C == 1 && GAM == 289) { dma_piece<21>(s, 0); }
  if constexpr (C == 1 && GAM == 290) { dma_piece<21>(s, 1); }
  if constexpr (C == 1 && GAM == 291) { dma_piece<21>(s, 2); }
  if constexpr (C == 1 && GAM == 292) { dma_piece<21>(s, 3); }
  if constexpr (C == 1 && GAM == 305) { dma_piece<22>(s, 0); }
  if constexpr (C == 1 && GAM == 306) { dma_piece<22>(s, 1); }
  if constexpr (C == 1 && GAM == 307) { dma_piece<22>(s, 2); }
  if constexpr (C == 1 && GAM == 308) { dma_piece<22>(s, 3); }
  if constexpr (C == 1 && GAM == 321) { dma_piece<23>(s, 0); }
  if constexpr (C == 1 && GAM == 322) { dma_piece<23>(s, 1); }
  if constexpr (C == 1 && GAM == 323) { dma_piece<23>(s, 2); }
  if constexpr (C == 1 && GAM == 324) { dma_piece<23>(s, 3); }
  if constexpr (C == 1 && GAM == 337) { dma_piece<24>(s, 0); }
  if constexpr (C == 1 && GAM == 338) { dma_piece<24>(s, 1); }
  if constexpr (C == 1 && GAM == 339) { dma_piece<24>(s, 2); }
  if constexpr (C == 1 && GAM == 340) { dma_piece<24>(s, 3); }
  if constexpr (C == 1 && GAM == 347) { warm_load(s, s.sWw, 0u); }
  if constexpr (C == 1 && GAM == 348) { warm_load(s, s.sWw, 1024u); }
  if constexpr (C == 1 && GAM == 349) { warm_load(s, s.sWw, 2048u); }
  if constexpr (C == 1 && GAM == 350) { warm_load(s, s.sWw, 3072u); }
  if constexpr (C == 1 && GAM == 353) { dma_piece<25>(s, 0); }
  if constexpr (C == 1 && GAM == 354) { dma_piece<25>(s, 1); }
  if constexpr (C == 1 && GAM == 355) { dma_piece<25>(s, 2); }
  if constexpr (C == 1 && GAM == 356) { dma_piece<25>(s, 3); }
  if constexpr (C == 1 && GAM == 369) { dma_piece<26>(s, 0); }
  if constexpr (C == 1 && GAM == 370) { dma_piece<26>(s, 1); }
  if constexpr (C == 1 && GAM == 371) { dma_piece<26>(s, 2); }
  if constexpr (C == 1 && GAM == 372) { dma_piece<26>(s, 3); }
  if constexpr (C == 1 && GAM == 395) { PIN(s.dummy); }
  if constexpr (C == 1 && GAM == 401) { dma_piece<27>(s, 0); }
  if constexpr (C == 1 && GAM == 402) { dma_piece<27>(s, 1); }
  if constexpr (C == 1 && GAM == 403) { dma_piece<27>(s, 2); }
  if constexpr (C == 1 && GAM == 404) { dma_piece<27>(s, 3); }
  if constexpr (C == 1 && GAM == 417) { dma_piece<28>(s, 0); }
  if constexpr (C == 1 && GAM == 418) { dma_piece<28>(s, 1); }
  if constexpr (C == 1 && GAM == 419) { dma_piece<28>(s, 2); }
  if constexpr (C == 1 && GAM == 420) { dma_piece<28>(s, 3); }
  if constexpr (C == 1 && GAM == 421) { dma_piece<29>(s, 0); }
  if constexpr (C == 1 && GAM == 422) { dma_piece<29>(s, 1); }
  if constexpr (C == 1 && GAM == 423) { dma_piece<29>(s, 2); }
  if constexpr (C == 1 && GAM == 424) { dma_piece<29>(s, 3); }
  if constexpr (C == 1 && GAM == 425) { dma_piece<30>(s, 0); }
  if constexpr (C == 1 && GAM == 426) { dma_piece<30>(s, 1); slab_store<0, 0>(s); }
  if constexpr (C == 1 && GAM == 427) { dma_piece<30>(s, 2); slab_store<0, 1>(s); }
  if constexpr (C == 1 && GAM == 428) { dma_piece<30>(s, 3); slab_store<0, 2>(s); }
  if constexpr (C == 1 && GAM == 429) { slab_store<0, 3>(s); }
  if constexpr (C == 1 && GAM == 433) { dma_piece<31>(s, 0); }
  if constexpr (C == 1 && GAM == 434) { dma_piece<31>(s, 1); }
  if constexpr (C == 1 && GAM == 435) { dma_piece<31>(s, 2); }
  if constexpr (C == 1 && GAM == 436) { dma_piece<31>(s, 3); }
  if constexpr (C == 1 && GAM == 458) { slab_store<1, 0>(s); }
  if constexpr (C == 1 && GAM == 459) { slab_store<1, 1>(s); }
  if constexpr (C == 1 && GAM == 460) { slab_store<1, 2>(s); }
  if constexpr (C == 1 && GAM == 461) { slab_store<1, 3>(s); }
  if constexpr (C == 1 && GAM == 490) { slab_store<2, 0>(s); }
  if constexpr (C == 1 && GAM == 491) { slab_store<2, 1>(s); }
  if constexpr (C == 1 && GAM == 492) { slab_store<2, 2>(s); }
  if constexpr (C == 1 && GAM == 493) { slab_store<2, 3>(s); }

}

template <int C, int P, int G> DI void gap(St& s) {
  if constexpr (P < 8) {
    constexpr int ks = G >> 2, dt = G & 3;
    if constexpr (P == 0 && ks == 0) s.acc[dt] = MFMA32(s.af[P & 1][G], s.xf[P & 1][ks], f32x16{});
    else s.acc[dt] = MFMA32(s.af[P & 1][G], s.xf[P & 1][ks], s.acc[dt]);
  } else if constexpr (is_k(P)) {
    constexpr int t = kv_idx(P), kt = G >> 3, d = G & 7;
    if constexpr (d == 0) s.s[t & 1][kt] = MFMA32(s.af[P & 1][G], s.qf[d], f32x16{});
    else s.s[t & 1][kt] = MFMA32(s.af[P & 1][G], s.qf[d], s.s[t & 1][kt]);
  } else if constexpr (is_v(P)) {
    constexpr int t = kv_idx(P), ks = G >> 2, dt = G & 3;
    const u32x4 pv = {s.pw[4 * ks], s.pw[4 * ks + 1], s.pw[4 * ks + 2], s.pw[4 * ks + 3]};
    if constexpr (t == 0 && ks == 0) s.acc[dt] = MFMA32(s.af[P & 1][G], __builtin_bit_cast(bf16x8, pv), f32x16{});
    else s.acc[dt] = MFMA32(s.af[P & 1][G], __builtin_bit_cast(bf16x8, pv), s.acc[dt]);
  } else {
    constexpr int c4 = (P - 24) >> 1, j = (P - 24) & 1, ks = G >> 1, nt = G & 1;
    const bf16x8 bfr = j ? s.of[ks] : s.qf[ks];
    f32x16& d = s.acc[2 * (c4 & 1) + nt];
    if constexpr (j == 0 && ks == 0) d = MFMA32(s.af[P & 1][G], bfr, f32x16{});
    else d = MFMA32(s.af[P & 1][G], bfr, d);
  }
  SBAR();
  if constexpr (G < 8) {
    constexpr int f0 = 4 + (3 * G + 1) / 2, f1 = 4 + (3 * (G + 1) + 1) / 2;
    read_frag<P, f0>(s);
    if constexpr (f1 - f0 == 2) read_frag<P, f0 + 1>(s);
  } else if constexpr (G >= 12 && P + 1 < NP) {
    read_frag<P + 1, G - 12>(s);
    if constexpr (P + 1 < 8) x_read<P + 1, G - 12>(s);
  }
  if constexpr (P == 8 && G < 7) {
    s.qf[G + 1] = __builtin_bit_cast(bf16x8, pack8(s.acc[(G + 1) >> 1], (G + 1) & 1, SCL2)); PIN(s.qf[G + 1]);
  } else if constexpr (P == 23) {
    if constexpr (G < 12) {
      exp1<7, 8 + 2 * G>(s); exp1<7, 9 + 2 * G>(s); PIN(s.s[1][(8 + 2 * G) >> 4]);
      if constexpr ((G & 3) != 0) { cvt1<7, 3 + G>(s); PIN(s.pw[3 + G]); }
      if constexpr ((G & 3) == 3) { cvt1<7, 4 + G>(s); PIN(s.pw[4 + G]); }
    }
    if constexpr (G >= 4) { sum1<7, 2 * (G - 4)>(s); sum1<7, 2 * (G - 4) + 1>(s); }
    if constexpr (G >= 12) { sum1<7, 24 + 2 * (G - 12)>(s); sum1<7, 25 + 2 * (G - 12)>(s); }
  } else
  if constexpr (P >= 9 && is_k(P)) {
    constexpr int t = kv_idx(P) - 1;
    exp1<t, 2 * G>(s); exp1<t, 2 * G + 1>(s);
    if constexpr (G >= 8 && G < 12) { cvt1<t, 2 * (G - 8)>(s); cvt1<t, 2 * (G - 8) + 1>(s); PIN(s.pw[2 * (G - 8)]); PIN(s.pw[2 * (G - 8) + 1]); }
    PIN(s.s[t & 1][G >> 3]);
  } else if constexpr (is_v(P)) {
    constexpr int t = kv_idx(P);
    if constexpr (G < 8) {
      sum1<t, 2 * G>(s); sum1<t, 2 * G + 1>(s);
      cvt1<t, 8 + G>(s); PIN(s.pw[8 + G]);
    } else if constexpr (t < 7 && G < 12) {
      max1<t + 1, 3 * (G - 8)>(s); sum1<t, 2 * G>(s); max1<t + 1, 3 * (G - 8) + 1>(s); sum1<t, 2 * G + 1>(s); max1<t + 1, 3 * (G - 8) + 2>(s);
    } else if constexpr (t < 7 && G < 14) {
      max1<t + 1, 12 + 2 * (G - 12)>(s); sum1<t, 2 * G>(s); max1<t + 1, 13 + 2 * (G - 12)>(s); sum1<t, 2 * G + 1>(s);
    } else {
      sum1<t, 2 * G>(s); sum1<t, 2 * G + 1>(s);
      if constexpr (t < 7 && G == 14) s.mx = rowmax_finish(s);
    }
  }
  if constexpr (G == BG && P + 1 < NP) {
    if constexpr (barwait(C, P) >= 0) wait_vm<(barwait(C, P) >= 0 ? barwait(C, P) : 0)>();
    barrier_lds();
  }
  if constexpr (P == 24 && G >= 11 && G < 15) {
    s.of[2 * (G - 11)] = *(lds_bf16x8*)(s.oex_r + (2 * (G - 11)) * 1024); s.of[2 * (G - 11) + 1] = *(lds_bf16x8*)(s.oex_r + (2 * (G - 11) + 1) * 1024);
  }
  vm_ops<C, P * 16 + G>(s);
  SBAR();
}
template <int C, int P, int... Gs> DI void phase_(St& s, iseq<Gs...>) { (gap<C, P, Gs>(s), ...); }
template <int C, int P> DI void phase(St& s) {
  phase_<C, P>(s, std::make_integer_sequence<int, 16>{});
  if constexpr (P == 7) {
    s.qf[0] = __builtin_bit_cast(bf16x8, pack8(s.acc[0], 0, SCL2));
    SBAR();
  } else if constexpr (P == 8) {
    float mx = fmaxf(fmaxf(s.s[0][0][0], s.s[0][0][1]), s.s[0][1][0]), my = fmaxf(fmaxf(s.s[0][0][2], s.s[0][0][3]), s.s[0][1][1]);
    mx = fmaxf(fmaxf(mx, s.s[0][1][2]), s.s[0][1][3]);
#pragma unroll
    for (int r = 4; r < 16; r += 4) {
      mx = fmaxf(fmaxf(mx, s.s[0][0][r]), s.s[0][0][r + 1]); my = fmaxf(fmaxf(my, s.s[0][0][r + 2]), s.s[0][0][r + 3]);
      mx = fmaxf(fmaxf(mx, s.s[0][1][r]), s.s[0][1][r + 1]); my = fmaxf(fmaxf(my, s.s[0][1][r + 2]), s.s[0][1][r + 3]);
    }
    s.mx = mx; s.my = my;
    const float g = rowmax_finish(s);
    s.m = 0.f; s.la = 0.f; s.lb = 0.f; s.mflag = false;
    if (__builtin_expect(__any(!(fabsf(g) <= RESC_THR)), 0)) {
      s.m = g; s.mflag = true;
      sub16(s.s[0][0], g); sub16(s.s[0][1], g);
    }
    SBAR();
  } else if constexpr (is_v(P) && P < 23) {
    constexpr int t1 = kv_idx(P) + 1;
    const float g = s.mx;
    if (__builtin_expect(s.mflag || __any(g > RESC_THR), 0)) {
      if (__any(g - s.m > RESC_THR)) {
        const float mn = fmaxf(s.m, g), f = __builtin_amdgcn_exp2f(s.m - mn);
        s.m = mn; s.la *= f; s.lb *= f;
        asm volatile("s_nop 7\n\ts_nop 7");
        mul16(s.acc[0], f); mul16(s.acc[1], f); mul16(s.acc[2], f); mul16(s.acc[3], f);
        asm volatile("s_nop 1");
      }
      s.mflag = true;
      sub16(s.s[t1 & 1][0], s.m); sub16(s.s[t1 & 1][1], s.m);
    }
    if constexpr (P == 22) {
#pragma unroll
      for (int r = 0; r < 8; ++r) s.s[1][0][r] = __builtin_amdgcn_exp2f(s.s[1][0][r]);
      cvt1<7, 0>(s); cvt1<7, 1>(s); cvt1<7, 2>(s); cvt1<7, 3>(s);
    }
    SBAR();
  } else if constexpr (P == 23) {
    float l = s.la + s.lb;
    { auto rr = __builtin_amdgcn_permlane32_swap(__float_as_uint(l), __float_as_uint(l), false, false);
      l = __uint_as_float(rr[0]) + __uint_as_float(rr[1]); }
    const float inv = 1.0f / l;
#pragma unroll
    for (int i = 0; i < 8; ++i) {
      const u32x4 v = pack8(s.acc[i >> 1], i & 1, inv);
      s.qf[i] = __builtin_bit_cast(bf16x8, v);
      *(lds_u32x4*)(s.oex_w + i * 1024) = v;
    }
    SBAR();
  }
    }
template <int C, int... Ps> DI void phases(St& s, iseq<Ps...>) { (phase<C, Ps>(s), ...); }
template <int C> DI void body(St& s, const srd_t& sF, unsigned& fold_out, unsigned vF, unsigned vack) {
  unsigned fold = bload4_sc1(sF, vF);
  u32x4 cpf;
  asm volatile("buffer_load_dwordx4 %0, %1, %2, 0 offen" : "=v"(cpf) : "v"(s.vcode), "s"(s.sC) : "memory");
  if constexpr (C == 0) {
  dma_piece<0>(s, 0); dma_piece<0>(s, 1); dma_piece<0>(s, 2); dma_piece<0>(s, 3); dma_piece<0>(s, 4); dma_piece<0>(s, 5); dma_piece<1>(s, 0); dma_piece<1>(s, 1); dma_piece<1>(s, 2); dma_piece<1>(s, 3); dma_piece<1>(s, 4); dma_piece<1>(s, 5); dma_piece<2>(s, 0); dma_piece<2>(s, 1); dma_piece<2>(s, 2); dma_piece<2>(s, 3); dma_piece<2>(s, 4); dma_piece<2>(s, 5);
  wait_vm<12>();
  }
  if constexpr (C == 1) {
  dma_piece<0>(s, 0); dma_piece<0>(s, 1); dma_piece<0>(s, 2); dma_piece<0>(s, 3); dma_piece<0>(s, 4); dma_piece<0>(s, 5); dma_piece<1>(s, 0); dma_piece<1>(s, 1); dma_piece<1>(s, 2); dma_piece<1>(s, 3); dma_piece<1>(s, 4); dma_piece<1>(s, 5); dma_piece<2>(s, 0); dma_piece<2>(s, 1); dma_piece<2>(s, 2); dma_piece<2>(s, 3); dma_piece<2>(s, 4); dma_piece<2>(s, 5);
  wait_vm<12>();
  }

  PIN(fold);
  PIN(cpf);
  fold_out = fold;
  bstore4_sc1(sF, vack, fold + ACK_K);
  barrier_lds();
  read_frag<0, 0>(s); read_frag<0, 1>(s); read_frag<0, 2>(s); read_frag<0, 3>(s);
  x_read<0, 0>(s); x_read<0, 1>(s); x_read<0, 2>(s); x_read<0, 3>(s);
  SBAR();
  phases<C>(s, std::make_integer_sequence<int, NP>{});
}

__global__ __launch_bounds__(512, 2) void fused_kernel(Args a) {
  __shared__ __attribute__((aligned(1024))) char smem[10 * TILE];
  const int tid = threadIdx.x, lane = tid & 63;
  const unsigned wv = __builtin_amdgcn_readfirstlane(tid >> 6), grp = wv >> 2, w4 = wv & 3;
  const int bid = blockIdx.x, g8 = bid & 7, r = bid >> 3;
  const int br = g8 >> 2, hp = (g8 & 3) * 4 + (r & 3), b = r >> 2, pair = br * 8 + b;
  const unsigned h = hp * 2 + grp;
  St s;
  const unsigned lds0 = (unsigned)(uintptr_t)(lds_ptr)smem;
  s.wv = wv; s.w4 = w4; s.grp = grp; s.ldsg = lds0 + grp * GREG; s.ldsx = lds0;
  s.rot = (b + 2 * (r & 3)) & 7;
  const size_t kvoff = ((size_t)(b * NH) + h) * (SK * DH * 2);
  s.sK = make_srd((const char*)a.k[br] + kvoff, SK * DH * 2);
  s.sV = make_srd((const char*)a.v[br] + kvoff, SK * DH * 2);
  s.sW = make_srd((const char*)a.wq[br] + h * 256, (DM * NH * DH * 2 - h * 256));
  s.sX = make_srd((const char*)a.x[br] + (size_t)b * SQ * DM * 2, SQ * DM * 2);
  s.sWo = make_srd((const char*)a.wo[br] + (size_t)hp * 256 * 1024 + grp * 512, (256 * 1024 - grp * 512));
  s.sWw = make_srd((const char*)a.wo[br] + (size_t)hp * 256 * 1024 + b * 32768 + wv * 4096, 4096);
  s.sR = make_srd((const char*)a.ws + (size_t)pair * 16 * SLAB, 16 * SLAB);
  s.sP = make_srd((const char*)a.ws + (size_t)(pair * 16 + hp) * SLAB + grp * (32 * SQ * 16), 32 * SQ * 16);
  const srd_t sF = make_srd((const char*)a.ws + WS_F, ACK_OFF + 16 * 16 * 128);
  const unsigned r8 = (lane >> 2) & 7, cbl = lane >> 5, c4 = lane & 3;
  s.vV = ((lane >> 2) & 3) * 256 + (lane >> 4) * 64 + c4 * 16;
  s.vK[0] = s.vV; s.vK[1] = s.vV ^ 16; s.vK[2] = s.vV ^ 32; s.vK[3] = s.vV ^ 48;
  s.vW = ((lane >> 2) & 3) * 8192 + (lane >> 4) * 64 + c4 * 16;
  s.vWo = r8 * 1024 + cbl * 64 + c4 * 16;
  s.vx0 = (lane >> 3) * 1024 + (((lane & 7) ^ (lane >> 4)) * 16);
  s.vx1 = (lane >> 3) * 1024 + (((lane & 7) ^ (4 + (lane >> 4))) * 16);
  const int hh = lane >> 5, q32 = lane & 31;
  {
    const int blk = (lane >> 4) & 1, q4 = (lane & 15) >> 2, p = lane & 3, ps = ((p & 1) << 1) | (p >> 1);
    const lds_ptr gb = (lds_ptr)smem + grp * GREG;
    s.trb = gb + (hh * 2048 + q4 * 64 + blk * 32 + ps * 8);
    const int rp = (q32 & ~12) | ((q32 & 4) << 1) | ((q32 & 8) >> 1);
    const int kb = 1024 * (rp >> 2) + 64 * (rp & 3) + 16 * (hh ^ ((rp >> 2) & 3));
    s.kb0 = gb + kb; s.kb1 = gb + (kb ^ 32);
    s.xrd = (lds_ptr)smem + 3 * TILE + (w4 * 32 + q32) * 128 + ((hh ^ ((q32 >> 1) & 7)) * 16);
    s.trbo = gb + (hh * 1024 + q4 * 64 + blk * 32 + ps * 8);
    s.vlin = lane * 16;
    s.pkw = gb + TILE + w4 * 4096 + lane * 16;
    s.oex_w = gb + 3 * TILE + w4 * 8192 + lane * 16;
    s.oex_r = (lds_ptr)smem + (grp ^ 1) * GREG + 3 * TILE + w4 * 8192 + lane * 16;
    const int q = w4 * 32 + q32;
    s.vP = q < SQ ? (unsigned)((hh * SQ + q) * 16) : OOB;
  }
  {
    unsigned long long pc; unsigned len;
    asm volatile("s_getpc_b64 %0\n.Lfk_begin:\n\ts_mov_b32 %1, .Lfk_end-.Lfk_begin" : "=s"(pc), "=s"(len));
    s.sC = make_srd((const void*)pc, len <= 0x20000u ? len : 0u);
    s.vcode = (unsigned)(((wv * 32 + r) * 64 + lane) * 16);
  }
  const unsigned vF = (unsigned)(pair * 256 + (lane & 15) * 4);
  unsigned fold;
  const unsigned vack = (wv == 0 && lane < 16) ? (unsigned)(ACK_OFF + (pair * 16 + hp) * 128 + lane * 4) : OOB;
  if (grp) body<1>(s, sF, fold, wv == 0 ? vF : OOB, vack); else body<0>(s, sF, fold, wv == 0 ? vF : OOB, vack);
  const unsigned vA = (unsigned)(ACK_OFF + (pair * 16 + (lane & 15)) * 128 + hp * 4);
  unsigned ackv = bload4_sc1(sF, wv == 0 ? vA : OOB);
  slab_store<3, 0>(s); slab_store<3, 1>(s); slab_store<3, 2>(s); slab_store<3, 3>(s);
  asm volatile("s_waitcnt vmcnt(0)" : "+v"(ackv) :: "memory");
  barrier_lds();
  if (wv == 0) {
    const unsigned own = __builtin_amdgcn_readlane(fold, hp);
    for (unsigned sp = 0; !__all(ackv == own + ACK_K) && sp < SPIN_CAP; ++sp) { __builtin_amdgcn_s_sleep(2); ackv = bload4_sc1_wait(sF, vA); }
    bstore4_sc1(sF, lane == hp ? vF : OOB, own + 1);
    for (unsigned sp = 0; sp < SPIN_CAP; ++sp) {
      const unsigned f = bload4_sc1_wait(sF, vF);
      if (__all(f != fold)) break;
      __builtin_amdgcn_s_sleep(4);
    }
  }
  barrier_lds();
  {
    const srd_t sR = s.sR;
    const int q = tid >> 2, no = tid & 3;
    const unsigned vr = q < SQ ? (unsigned)(((hp * 4 + no) * SQ + q) * 16) : OOB;
    u32x4 v[16];
#pragma unroll
    for (int i = 0; i < 16; ++i) v[i] = __builtin_amdgcn_raw_buffer_load_b128(sR, vr, i * SLAB, 16);
    float acc[8] = {};
#pragma unroll
    for (int i = 0; i < 16; ++i)
#pragma unroll
      for (int d = 0; d < 4; ++d) { acc[2 * d] += __uint_as_float(v[i][d] << 16); acc[2 * d + 1] += __uint_as_float(v[i][d] & 0xffff0000u); }
    if (q < SQ)
      *(u32x4*)((char*)a.out + ((size_t)(b * SQ + q) * 1024 + br * 512 + hp * 32 + no * 8) * 2) =
          u32x4{cvtpk(acc[0], acc[1]), cvtpk(acc[2], acc[3]), cvtpk(acc[4], acc[5]), cvtpk(acc[6], acc[7])};
  }
  asm volatile(".Lfk_end:");
}

extern "C" void kernel_launch(void* const* d_in, const int* in_sizes, int n_in,
                              void* d_out, int out_size, void* d_ws, size_t ws_size,
                              hipStream_t stream) {
  Args a;
  a.x[0] = d_in[0]; a.k[0] = d_in[1]; a.v[0] = d_in[2]; a.wq[0] = d_in[3]; a.wo[0] = d_in[4];
  a.x[1] = d_in[5]; a.k[1] = d_in[6]; a.v[1] = d_in[7]; a.wq[1] = d_in[8]; a.wo[1] = d_in[9];
  a.ws = d_ws; a.out = d_out;
  fused_kernel<<<256, 512, 0, stream>>>(a);
}
```
